```python
import torch

def _softmax(x, axis=-1):
    return torch.softmax(x, dim=axis)
def _gelu(x):
    return torch.nn.functional.gelu(x, approximate="tanh")
def _silu(x):
    return torch.nn.functional.silu(x)
def _sigmoid(x):
    return torch.sigmoid(x)
def _layernorm(x, axis=-1, eps=1e-5):
    m = x.mean(dim=axis, keepdim=True)
    v = x.var(dim=axis, keepdim=True, unbiased=False)
    return (x - m) / torch.sqrt(v + eps)
def _scatter_add(tgt, idx, src, axis=0):
    return tgt.clone().index_add_(axis, idx, src)
def _segment_sum(src, seg, n):
    out = torch.zeros((n,) + src.shape[1:], dtype=src.dtype)
    return out.index_add_(0, seg, src)
def _pad(x, pw):
    flat = tuple(v for pair in reversed(pw) for v in pair)
    return torch.nn.functional.pad(x, flat)

kernel_name = 'recurrent_scan__gqa_block_cached__glu_gated_reglu__decode'

def setup_inputs():

    _w = (lambda a: (lambda t: t.to(torch.bfloat16) if t.is_floating_point() else t)(torch.as_tensor(a)))
    def _A_setup():
        B, Sq, D = 8, 1, 512
        Hq, Hkv, Sk, Dh = 32, 8, 8192, 128
        assert Hq % Hkv == 0, (Hq, Hkv)
        return {
            "x": _w((lambda *s: torch.randn(*s, dtype=torch.bfloat16) * 0.2)(B, Sq, D)),
            "k_cache": _w((lambda *s: torch.randn(*s, dtype=torch.bfloat16) * 1.6)(B, Hkv, Sk, Dh)),
            "v_cache": _w((lambda *s: torch.randn(*s, dtype=torch.bfloat16) * 1.6)(B, Hkv, Sk, Dh)),
            "wq": _w((lambda *s: torch.randn(*s, dtype=torch.bfloat16) * 1.6)(D, Hq * Dh) * (D ** -0.5)),
            "wo": _w((lambda *s: torch.randn(*s, dtype=torch.bfloat16) * 1.6)(Hq * Dh, D) * ((Hq * Dh) ** -0.5)),
        }
    _A_in = _A_setup()

    def _B_setup():
        B, S, D, F = 8, 1, 512, 1792
        return {
            "x": _w((lambda *s: torch.randn(*s, dtype=torch.bfloat16) * 0.2)(B, S, D)),
            "W_gate": _w((lambda *s: torch.randn(*s, dtype=torch.bfloat16) * 0.8)(D, F) * (2.0 / D) ** 0.5),
            "W_up": _w((lambda *s: torch.randn(*s, dtype=torch.bfloat16) * 0.8)(D, F) * (2.0 / D) ** 0.5),
            "W_down": _w((lambda *s: torch.randn(*s, dtype=torch.bfloat16) * 0.8)(F, D) * (1.0 / F) ** 0.5),
        }
    _B_in = _B_setup()

    return {
        "A_x": _A_in["x"],
        "A_k_cache": _A_in["k_cache"],
        "A_v_cache": _A_in["v_cache"],
        "A_wq": _A_in["wq"],
        "A_wo": _A_in["wo"],
        "B_x": _B_in["x"],
        "B_W_gate": _B_in["W_gate"],
        "B_W_up": _B_in["W_up"],
        "B_W_down": _B_in["W_down"],
    }

def reference(A_x, A_k_cache, A_v_cache, A_wq, A_wo, B_x, B_W_gate, B_W_up, B_W_down):
    def _A_ref(x, k_cache, v_cache, wq, wo):
        B, Sq, D = x.shape
        Hq, Dh = 32, 128
        Hkv = k_cache.shape[1]
        rep = Hq // Hkv
        q = (lambda x, *d: x.permute(*d))((x @ wq).reshape(B, Sq, Hq, Dh), 0, 2, 1, 3)
        k = (lambda x, n, axis: torch.repeat_interleave(x, n, dim=axis))(k_cache, rep, axis=1)
        v = (lambda x, n, axis: torch.repeat_interleave(x, n, dim=axis))(v_cache, rep, axis=1)
        scores = torch.einsum("bhqd,bhkd->bhqk", q, k) * (Dh ** -0.5)
        p = _softmax(scores, axis=-1)
        o = torch.einsum("bhqk,bhkd->bhqd", p, v)
        return (lambda x, *d: x.permute(*d))(o, 0, 2, 1, 3).reshape(B, Sq, Hq * Dh) @ wo

    def _B_ref(x, W_gate, W_up, W_down):
        gate = torch.einsum("bsd,df->bsf", x, W_gate)
        up = torch.einsum("bsd,df->bsf", x, W_up)
        gate = gate * (lambda x, dt: x.to(dt))(gate > 0, gate.dtype)
        h = gate * up
        return torch.einsum("bsf,fd->bsd", h, W_down)

    x = A_x
    xn = _layernorm(x, axis=-1)
    s = _A_ref(xn, A_k_cache, A_v_cache, A_wq, A_wo)
    g = _silu(_B_ref(xn, B_W_gate, B_W_up, B_W_down))
    return x + s * g
```

```cpp
#include <hip/hip_runtime.h>
#include <hip/hip_bf16.h>
#include <stdint.h>

#define GAS __attribute__((address_space(1)))
#define LAS __attribute__((address_space(3)))
#define RLX_AGENT __ATOMIC_RELAXED, __HIP_MEMORY_SCOPE_AGENT
typedef unsigned v4u __attribute__((ext_vector_type(4)));
typedef unsigned v2u __attribute__((ext_vector_type(2)));
typedef float f32x4 __attribute__((ext_vector_type(4)));
typedef float f32x2 __attribute__((ext_vector_type(2)));
typedef short bf16x8 __attribute__((ext_vector_type(8)));
typedef short s16x4 __attribute__((ext_vector_type(4)));
typedef __bf16 bf16x2 __attribute__((ext_vector_type(2)));
typedef unsigned long long u64;
typedef GAS u64 gu64;
typedef GAS unsigned gu32;

namespace {
constexpr int NB = 8, DM = 512, HQ = 32, HKV = 8, SK = 8192, DH = 128, FF = 1792;
constexpr int NWG = 256, NTHR = 512, NWAVE = 8;
constexpr int NTILE = 8;
constexpr float SCALE = 0.08838834764831845f;
constexpr float L2E = 1.4426950408889634f;
constexpr unsigned SPIN_CAP = 1u << 18;

#define KV_AUX 2
#define W_AUX 2

constexpr size_t WS_EPOCH = 0;
constexpr size_t WS_QG = 4096;
constexpr size_t WS_HG = WS_QG + (size_t)NB * 2048 * 8;
constexpr int PSTR = 272;
constexpr size_t WS_PG = WS_HG + (size_t)NB * 896 * 8;
constexpr size_t WS_END = WS_PG + (size_t)NWG * PSTR * 8;

constexpr int XN_STR = 1040;
constexpr int L_XN = 0;
constexpr int L_WQ = 8448;
constexpr int L_GU = L_WQ + 16384;
constexpr int QS_STR = 272;
constexpr int L_QS = L_GU + 16384;
constexpr int L_MRG = L_QS + 1152;
constexpr int L_ML = L_MRG + 16384;
constexpr int L_FIN = L_ML + 256;
constexpr int L_END = L_FIN + 1024;

__device__ __forceinline__ f32x4 mfma(v4u a, v4u b, f32x4 c) {
  return __builtin_amdgcn_mfma_f32_16x16x32_bf16(__builtin_bit_cast(bf16x8, a), __builtin_bit_cast(bf16x8, b), c, 0, 0, 0);
}
__device__ __forceinline__ unsigned cvtpk(float lo, float hi) {
  f32x2 v = {lo, hi};
  return __builtin_bit_cast(unsigned, __builtin_convertvector(v, bf16x2));
}
__device__ __forceinline__ float bflo(unsigned u) { return __builtin_bit_cast(float, u << 16); }
__device__ __forceinline__ float bfhi(unsigned u) { return __builtin_bit_cast(float, u & 0xffff0000u); }
__device__ __forceinline__ float rbf(float x) { return bflo(cvtpk(x, x)); }
__device__ __forceinline__ float dot2(unsigned a, unsigned b, float c) {
  return __builtin_amdgcn_fdot2_f32_bf16(__builtin_bit_cast(bf16x2, a), __builtin_bit_cast(bf16x2, b), c, false);
}
template <int CTRL> __device__ __forceinline__ float dpp(float x) {
  return __builtin_bit_cast(float, __builtin_amdgcn_mov_dpp(__builtin_bit_cast(int, x), CTRL, 0xf, 0xf, true));
}
constexpr int ROR1 = 0x121, ROR2 = 0x122, ROR4 = 0x124, ROR8 = 0x128;
__device__ __forceinline__ float xrow_max(float x) {
  auto s = __builtin_amdgcn_permlane16_swap(__float_as_uint(x), __float_as_uint(x), false, false);
  x = fmaxf(__uint_as_float(s[0]), __uint_as_float(s[1]));
  auto t = __builtin_amdgcn_permlane32_swap(__float_as_uint(x), __float_as_uint(x), false, false);
  return fmaxf(__uint_as_float(t[0]), __uint_as_float(t[1]));
}
__device__ __forceinline__ float xrow_sum(float x) {
  auto s = __builtin_amdgcn_permlane16_swap(__float_as_uint(x), __float_as_uint(x), false, false);
  x = __uint_as_float(s[0]) + __uint_as_float(s[1]);
  auto t = __builtin_amdgcn_permlane32_swap(__float_as_uint(x), __float_as_uint(x), false, false);
  return __uint_as_float(t[0]) + __uint_as_float(t[1]);
}
__device__ __forceinline__ float row_sum(float x) {
  x += dpp<ROR8>(x); x += dpp<ROR4>(x); x += dpp<ROR2>(x); x += dpp<ROR1>(x); return x;
}
__device__ __forceinline__ float wave_sum(float x) { return xrow_sum(row_sum(x)); }
__device__ __forceinline__ v4u ldw(const void* p) {
  return __builtin_nontemporal_load((const GAS v4u*)p);
}
__device__ __forceinline__ void st_gran(gu64* g, unsigned tag, unsigned v) {
  __hip_atomic_store(g, ((u64)tag << 32) | (u64)v, RLX_AGENT);
}
__device__ __forceinline__ u64 ld_gran(gu64* g) { return __hip_atomic_load(g, RLX_AGENT); }

__device__ __forceinline__ f32x4 small_gemm(LAS unsigned char* L, int img, int lane) {
  const int g = lane >> 4, q4 = (lane & 15) >> 2, p = lane & 3, c = lane & 15;
  LAS unsigned char* wa = L + img + (8 * g + q4) * 32 + 8 * p;
  LAS unsigned char* xb = L + L_XN + (c & 7) * XN_STR + g * 16;
  f32x4 a0 = {0.f, 0.f, 0.f, 0.f}, a1 = {0.f, 0.f, 0.f, 0.f};
#pragma unroll
  for (int ks = 0; ks < 16; ++ks) {
    const s16x4 lo = __builtin_amdgcn_ds_read_tr16_b64_v4i16((LAS s16x4*)(wa + ks * 1024));
    const s16x4 hi = __builtin_amdgcn_ds_read_tr16_b64_v4i16((LAS s16x4*)(wa + ks * 1024 + 128));
    const v2u l2 = __builtin_bit_cast(v2u, lo), h2 = __builtin_bit_cast(v2u, hi);
    const v4u a = {l2.x, l2.y, h2.x, h2.y};
    const v4u b = *(LAS v4u*)(xb + ks * 64);
    if (ks & 1) a1 = mfma(a, b, a1); else a0 = mfma(a, b, a0);
  }
  return a0 + a1;
}

__device__ __forceinline__ void reduce16_store(const float (&v)[16], LAS float* dst, int lane) {
  float y[8], z[4];
#pragma unroll
  for (int a = 0; a < 8; ++a) {
    auto s = __builtin_amdgcn_permlane32_swap(__float_as_uint(v[2 * a]), __float_as_uint(v[2 * a + 1]), false, false);
    y[a] = __uint_as_float(s[0]) + __uint_as_float(s[1]);
  }
#pragma unroll
  for (int a = 0; a < 4; ++a) {
    auto s = __builtin_amdgcn_permlane16_swap(__float_as_uint(y[2 * a]), __float_as_uint(y[2 * a + 1]), false, false);
    z[a] = row_sum(__uint_as_float(s[0]) + __uint_as_float(s[1]));
  }
  const int row = lane >> 4, pr = ((row & 1) << 1) | (row >> 1);
  if ((lane & 15) == 0) {
#pragma unroll
    for (int a = 0; a < 4; ++a) dst[4 * a + pr] = z[a];
  }
}
}

__global__ void __launch_bounds__(NTHR, 2)
fused_decode_kernel(const unsigned short* __restrict__ x, const unsigned short* __restrict__ Kc, const unsigned short* __restrict__ Vc,
                    const unsigned short* __restrict__ wq, const unsigned short* __restrict__ wo, const unsigned short* __restrict__ wg,
                    const unsigned short* __restrict__ wu, const unsigned short* __restrict__ wd, unsigned short* __restrict__ out,
                    unsigned char* __restrict__ ws) {
  __shared__ __attribute__((aligned(16))) unsigned char lds_raw[L_END];
  LAS unsigned char* L = (LAS unsigned char*)lds_raw;
  const int tid = threadIdx.x, lane = tid & 63, w = __builtin_amdgcn_readfirstlane(tid >> 6);
  const int g = lane >> 4, c = lane & 15;
  const int j = blockIdx.x, xc = j & 7, idx = j >> 3;
  const int kvh = xc, b = idx >> 2, sp = idx & 3;
  const int cb = 4 * xc + (idx & 3);

  const unsigned epoch = *(const __attribute__((address_space(4))) unsigned*)(ws + WS_EPOCH);
  const v4u xv = *(const GAS v4u*)(x + w * DM + lane * 8);
  const int n0 = 512 * xc + 16 * idx;
  const v4u wq0 = ldw(wq + (size_t)(tid >> 1) * (HQ * DH) + n0 + (tid & 1) * 8);
  const v4u wq1 = ldw(wq + (size_t)(256 + (tid >> 1)) * (HQ * DH) + n0 + (tid & 1) * 8);
  const bool has_ff = idx < 28;
  const int f0 = 8 * (28 * xc + (has_ff ? idx : 0));
  const v4u g0 = ldw(wg + (size_t)tid * FF + f0);
  const v4u u0 = ldw(wu + (size_t)tid * FF + f0);
  __builtin_amdgcn_sched_barrier(0);

  const size_t kvbase = ((size_t)(b * HKV + kvh) * SK + sp * 256 + w * 32) * (DH * 2);
  const __amdgpu_buffer_rsrc_t rk = __builtin_amdgcn_make_buffer_rsrc((void*)((const char*)Kc + kvbase), 0, 0x7fffffff, 0x00020000);
  const __amdgpu_buffer_rsrc_t rv = __builtin_amdgcn_make_buffer_rsrc((void*)((const char*)Vc + kvbase), 0, 0x7fffffff, 0x00020000);
  const int nrec0 = (w == 0) ? 0 : 0x7fffffff;
  const __amdgpu_buffer_rsrc_t rk0 = __builtin_amdgcn_make_buffer_rsrc((void*)((const char*)Kc + kvbase), 0, nrec0, 0x00020000);
  const __amdgpu_buffer_rsrc_t rv0 = __builtin_amdgcn_make_buffer_rsrc((void*)((const char*)Vc + kvbase), 0, nrec0, 0x00020000);
  const int koff = c * 256 + g * 16;
  const int voff = g * 1024 + c * 16;
  v4u kb[2][8], vb[2][8];
#define ISSUE_K(RS, d, i) { _Pragma("unroll") for (int u = 0; u < 8; ++u) kb[d][u] = __builtin_amdgcn_raw_buffer_load_b128(RS, koff + (u >> 2) * 4096 + (u & 3) * 64, (i) * (1024 * 256), KV_AUX); __builtin_amdgcn_sched_barrier(0); }
#define ISSUE_V(RS, d, i) { _Pragma("unroll") for (int u = 0; u < 8; ++u) vb[d][u] = __builtin_amdgcn_raw_buffer_load_b128(RS, voff + (u >> 2) * 4096 + (u & 3) * 256, (i) * (1024 * 256), KV_AUX); __builtin_amdgcn_sched_barrier(0); }
  ISSUE_K(rk0, 0, 0) ISSUE_V(rv0, 0, 0) ISSUE_K(rk0, 1, 1) ISSUE_V(rv0, 1, 1)

  const unsigned tag = epoch + 1u;
  {
    float xf[8];
#pragma unroll
    for (int k = 0; k < 4; ++k) { xf[2 * k] = bflo(xv[k]); xf[2 * k + 1] = bfhi(xv[k]); }
    float s = 0.f;
#pragma unroll
    for (int k = 0; k < 8; ++k) s += xf[k];
    const float mean = wave_sum(s) * (1.0f / DM);
    float q2 = 0.f;
#pragma unroll
    for (int k = 0; k < 8; ++k) { const float d = xf[k] - mean; q2 = fmaf(d, d, q2); }
    const float var = wave_sum(q2) * (1.0f / DM);
    const float mb = rbf(mean), den = rbf(sqrtf(rbf(rbf(var) + 1e-5f)));
    v4u o;
#pragma unroll
    for (int k = 0; k < 4; ++k) o[k] = cvtpk(rbf(xf[2 * k] - mb) / den, rbf(xf[2 * k + 1] - mb) / den);
    *(LAS v4u*)(L + L_XN + w * XN_STR + lane * 16) = o;
  }
  *(LAS v4u*)(L + L_WQ + tid * 16) = wq0;
  *(LAS v4u*)(L + L_WQ + 8192 + tid * 16) = wq1;
  *(LAS v4u*)(L + L_GU + tid * 32) = g0;
  *(LAS v4u*)(L + L_GU + tid * 32 + 16) = u0;
  __syncthreads();

  if (w == 0) {
    const f32x4 qd = small_gemm(L, L_WQ, lane);
    if (c < 8) {
      gu64* qg = (gu64*)(ws + WS_QG) + c * 2048 + (n0 >> 1) + 2 * g;
      st_gran(qg, tag, cvtpk(qd[0], qd[1]));
      st_gran(qg + 1, tag, cvtpk(qd[2], qd[3]));
    }
    gu64* qs = (gu64*)(ws + WS_QG) + b * 2048 + kvh * 256 + lane * 4;
    u64 q0, q1, q2, q3;
    for (unsigned spins = 0;; ++spins) {
      q0 = ld_gran(qs); q1 = ld_gran(qs + 1); q2 = ld_gran(qs + 2); q3 = ld_gran(qs + 3);
      const bool ok = (unsigned)(q0 >> 32) == tag && (unsigned)(q1 >> 32) == tag && (unsigned)(q2 >> 32) == tag && (unsigned)(q3 >> 32) == tag;
      if (__all(ok) || spins > SPIN_CAP) break;
      __builtin_amdgcn_s_sleep(1);
    }
    const v4u qv = {(unsigned)q0, (unsigned)q1, (unsigned)q2, (unsigned)q3};
    *(LAS v4u*)(L + L_QS + g * QS_STR + c * 16) = qv;
    __builtin_amdgcn_sched_barrier(0);
    ISSUE_K(rk, 0, 0) ISSUE_V(rv, 0, 0) ISSUE_K(rk, 1, 1) ISSUE_V(rv, 1, 1)
  } else if (w == 1) {
    const f32x4 gd = small_gemm(L, L_GU, lane);
    float hv[4];
#pragma unroll
    for (int i = 0; i < 4; ++i) {
      const float mine = rbf(gd[i]);
      auto s = __builtin_amdgcn_permlane32_swap(__float_as_uint(mine), __float_as_uint(mine), false, false);
      const float gt = __uint_as_float(s[0]), up = __uint_as_float(s[1]);
      hv[i] = (gt > 0.f ? gt : 0.f * gt) * up;
    }
    if (has_ff && c < 8 && g < 2) {
      gu64* hg = (gu64*)(ws + WS_HG) + c * 896 + (f0 >> 1) + 2 * g;
      st_gran(hg, tag, cvtpk(hv[0], hv[1]));
      st_gran(hg + 1, tag, cvtpk(hv[2], hv[3]));
    }
  }
  __syncthreads();

  v4u qf[4];
#pragma unroll
  for (int kk = 0; kk < 4; ++kk) qf[kk] = *(LAS v4u*)(L + L_QS + (c & 3) * QS_STR + kk * 64 + g * 16);
  f32x4 acc[8];
#pragma unroll
  for (int t = 0; t < 8; ++t) acc[t] = f32x4{0.f, 0.f, 0.f, 0.f};
  float m = -INFINITY, l = 0.f;

  v4u wor[4][2][2], wdr[2][2][2];
  const unsigned short* wop = wo + (size_t)(512 * w + 2 * lane) * DM + 16 * cb;
  const int hpair1 = (lane < 48) ? lane + 64 : lane;
  const unsigned short* wdp0 = wd + (size_t)(224 * w + 2 * lane) * DM + 16 * cb;
  const unsigned short* wdp1 = wd + (size_t)(224 * w + 2 * hpair1) * DM + 16 * cb;

#pragma unroll
  for (int i = 0; i < NTILE; ++i) {
    const int d = i & 1;
    f32x4 s0 = {0.f, 0.f, 0.f, 0.f}, s1 = {0.f, 0.f, 0.f, 0.f};
#pragma unroll
    for (int kk = 0; kk < 4; ++kk) { s0 = mfma(kb[d][kk], qf[kk], s0); s1 = mfma(kb[d][4 + kk], qf[kk], s1); }
    __builtin_amdgcn_sched_barrier(0);
    if (i + 2 < NTILE) { ISSUE_K(rk, d, i + 2) }
    else if (i == NTILE - 2) {
#pragma unroll
      for (int r = 0; r < 2; ++r)
#pragma unroll
        for (int h = 0; h < 2; ++h) { wdr[0][r][h] = ldw(wdp0 + r * DM + h * 8); wdr[1][r][h] = ldw(wdp1 + r * DM + h * 8); }
      __builtin_amdgcn_sched_barrier(0);
    } else {
#pragma unroll
      for (int ii = 2; ii < 4; ++ii)
#pragma unroll
        for (int r = 0; r < 2; ++r)
#pragma unroll
          for (int h = 0; h < 2; ++h) wor[ii][r][h] = ldw(wop + (size_t)(128 * ii + r) * DM + h * 8);
      __builtin_amdgcn_sched_barrier(0);
    }
    float r[8];
    {
      const unsigned a0 = cvtpk(s0[0], s0[1]), a1 = cvtpk(s0[2], s0[3]), a2 = cvtpk(s1[0], s1[1]), a3 = cvtpk(s1[2], s1[3]);
      const unsigned b0 = cvtpk(bflo(a0) * SCALE, bfhi(a0) * SCALE), b1 = cvtpk(bflo(a1) * SCALE, bfhi(a1) * SCALE);
      const unsigned b2 = cvtpk(bflo(a2) * SCALE, bfhi(a2) * SCALE), b3 = cvtpk(bflo(a3) * SCALE, bfhi(a3) * SCALE);
      r[0] = bflo(b0); r[1] = bfhi(b0); r[2] = bflo(b1); r[3] = bfhi(b1); r[4] = bflo(b2); r[5] = bfhi(b2); r[6] = bflo(b3); r[7] = bfhi(b3);
    }
    float mt = fmaxf(fmaxf(fmaxf(r[0], r[1]), fmaxf(r[2], r[3])), fmaxf(fmaxf(r[4], r[5]), fmaxf(r[6], r[7])));
    const float mn = fmaxf(m, xrow_max(mt));
    if (__any(mn > m)) {
      const float alpha = __builtin_amdgcn_exp2f((m - mn) * L2E);
      l *= alpha;
#pragma unroll
      for (int t = 0; t < 8; ++t) acc[t] *= alpha;
      m = mn;
    }
    const float nm = -m * L2E;
    float p[8];
#pragma unroll
    for (int k = 0; k < 8; ++k) p[k] = __builtin_amdgcn_exp2f(fmaf(r[k], L2E, nm));
    l += ((p[0] + p[1]) + (p[2] + p[3])) + ((p[4] + p[5]) + (p[6] + p[7]));
    const v4u pf = {cvtpk(p[0], p[1]), cvtpk(p[2], p[3]), cvtpk(p[4], p[5]), cvtpk(p[6], p[7])};
#pragma unroll
    for (int t = 0; t < 8; ++t) {
      const unsigned sel = (t & 1) ? 0x07060302u : 0x05040100u;
      v4u a;
#pragma unroll
      for (int mm = 0; mm < 4; ++mm) a[mm] = __builtin_amdgcn_perm(vb[d][2 * mm + 1][t >> 1], vb[d][2 * mm][t >> 1], sel);
      acc[t] = mfma(a, pf, acc[t]);
    }
    __builtin_amdgcn_sched_barrier(0);
    if (i + 2 < NTILE) { ISSUE_V(rv, d, i + 2) }
    else if (i == NTILE - 2) {
#pragma unroll
      for (int ii = 0; ii < 2; ++ii)
#pragma unroll
        for (int r2 = 0; r2 < 2; ++r2)
#pragma unroll
          for (int h = 0; h < 2; ++h) wor[ii][r2][h] = ldw(wop + (size_t)(128 * ii + r2) * DM + h * 8);
      __builtin_amdgcn_sched_barrier(0);
    }
  }

  l = xrow_sum(l);
  if (c < 4) {
    LAS float* mr = (LAS float*)(L + L_MRG) + (w * 4 + c) * 128 + 32 * g;
#pragma unroll
    for (int i = 0; i < 4; ++i) {
      *(LAS f32x4*)(mr + 8 * i) = f32x4{acc[0][i], acc[1][i], acc[2][i], acc[3][i]};
      *(LAS f32x4*)(mr + 8 * i + 4) = f32x4{acc[4][i], acc[5][i], acc[6][i], acc[7][i]};
    }
    if (g == 0) *(LAS f32x2*)(L + L_ML + (w * 4 + c) * 8) = f32x2{m, l};
  }
  __syncthreads();
  if (w < 4) {
    f32x2 ml[8];
    float M = -INFINITY;
#pragma unroll
    for (int ww = 0; ww < 8; ++ww) { ml[ww] = *(LAS f32x2*)(L + L_ML + (ww * 4 + w) * 8); M = fmaxf(M, ml[ww].x); }
    float Ls = 0.f, o0 = 0.f, o1 = 0.f;
#pragma unroll
    for (int ww = 0; ww < 8; ++ww) {
      const float e = __builtin_amdgcn_exp2f((ml[ww].x - M) * L2E);
      const f32x2 a = *(LAS f32x2*)(L + L_MRG + ((ww * 4 + w) * 128 + 2 * lane) * 4);
      Ls = fmaf(e, ml[ww].y, Ls); o0 = fmaf(e, a.x, o0); o1 = fmaf(e, a.y, o1);
    }
    const float inv = 1.0f / Ls;
    gu64* pg = (gu64*)(ws + WS_PG) + (size_t)((b * HKV + kvh) * 4 + sp) * PSTR;
    st_gran(pg + w * 64 + lane, tag, cvtpk(o0 * inv, o1 * inv));
    if (lane == 0) st_gran(pg + 256 + w, tag, __float_as_uint(fmaf(M, L2E, __builtin_amdgcn_logf(Ls))));
  }

  LAS float* fin = (LAS float*)(L + L_FIN) + w * 32;
  {
    gu64* hs = (gu64*)(ws + WS_HG) + b * 896 + 112 * w;
    u64 h0, h1;
    for (unsigned spins = 0;; ++spins) {
      h0 = ld_gran(hs + lane); h1 = ld_gran(hs + hpair1);
      const bool ok = (unsigned)(h0 >> 32) == tag && (unsigned)(h1 >> 32) == tag;
      if (__all(ok) || spins > SPIN_CAP) break;
      __builtin_amdgcn_s_sleep(1);
    }
    const unsigned hp0 = (unsigned)h0, hp1 = lane < 48 ? (unsigned)h1 : 0u;
    float dv[16];
#pragma unroll
    for (int cc = 0; cc < 16; ++cc) {
      const unsigned sel = (cc & 1) ? 0x07060302u : 0x05040100u;
      const unsigned w0 = __builtin_amdgcn_perm(wdr[0][1][cc >> 3][(cc >> 1) & 3], wdr[0][0][cc >> 3][(cc >> 1) & 3], sel);
      const unsigned w1 = __builtin_amdgcn_perm(wdr[1][1][cc >> 3][(cc >> 1) & 3], wdr[1][0][cc >> 3][(cc >> 1) & 3], sel);
      dv[cc] = dot2(hp1, w1, dot2(hp0, w0, 0.f));
    }
    reduce16_store(dv, fin + 16, lane);
  }
  unsigned wp[4][16];
#pragma unroll
  for (int i = 0; i < 4; ++i)
#pragma unroll
    for (int cc = 0; cc < 16; ++cc)
      wp[i][cc] = __builtin_amdgcn_perm(wor[i][1][cc >> 3][(cc >> 1) & 3], wor[i][0][cc >> 3][(cc >> 1) & 3], (cc & 1) ? 0x07060302u : 0x05040100u);
  __builtin_amdgcn_sched_barrier(0);
  {
    gu64* ps = (gu64*)(ws + WS_PG) + (size_t)((b * HKV + w) * 4) * PSTR;
    u64 gr[4][4], ls;
    for (unsigned spins = 0;; ++spins) {
      bool ok = true;
      ls = ld_gran(ps + ((lane >> 2) & 3) * PSTR + 256 + (lane & 3));
#pragma unroll
      for (int s = 0; s < 4; ++s)
#pragma unroll
        for (int i = 0; i < 4; ++i) gr[s][i] = ld_gran(ps + s * PSTR + i * 64 + lane);
      ok = (unsigned)(ls >> 32) == tag;
#pragma unroll
      for (int s = 0; s < 4; ++s)
#pragma unroll
        for (int i = 0; i < 4; ++i) ok &= (unsigned)(gr[s][i] >> 32) == tag;
      if (__all(ok) || spins > SPIN_CAP) break;
      __builtin_amdgcn_s_sleep(1);
    }
    const float lse = __uint_as_float((unsigned)ls);
    float mx = fmaxf(lse, dpp<ROR4>(lse)); mx = fmaxf(mx, dpp<ROR8>(mx));
    const float e = __builtin_amdgcn_exp2f(lse - mx);
    float se = e + dpp<ROR4>(e); se += dpp<ROR8>(se);
    const float wgt = e / se;
    float sv[16];
#pragma unroll
    for (int cc = 0; cc < 16; ++cc) sv[cc] = 0.f;
#pragma unroll
    for (int i = 0; i < 4; ++i) {
      float o0 = 0.f, o1 = 0.f;
#pragma unroll
      for (int s = 0; s < 4; ++s) {
        const float ww = __builtin_bit_cast(float, __builtin_amdgcn_readlane(__builtin_bit_cast(int, wgt), s * 4 + i));
        const unsigned pv = (unsigned)gr[s][i];
        o0 = fmaf(ww, bflo(pv), o0); o1 = fmaf(ww, bfhi(pv), o1);
      }
      const unsigned op = cvtpk(o0, o1);
#pragma unroll
      for (int cc = 0; cc < 16; ++cc) sv[cc] = dot2(op, wp[i][cc], sv[cc]);
    }
    reduce16_store(sv, fin, lane);
  }
  __syncthreads();
  if (tid < 16) {
    LAS float* f = (LAS float*)(L + L_FIN);
    float s = 0.f, dd = 0.f;
#pragma unroll
    for (int ww = 0; ww < 8; ++ww) { s += f[ww * 32 + tid]; dd += f[ww * 32 + 16 + tid]; }
    const float sb = rbf(s), db = rbf(dd);
    const float gt = rbf(db / (1.0f + __expf(-db)));
    const float xo = bflo((unsigned)x[b * DM + 16 * cb + tid]);
    const float o = xo + rbf(sb * gt);
    out[b * DM + 16 * cb + tid] = (unsigned short)(cvtpk(o, o) & 0xffffu);
    if (j == 0 && tid == 0) __hip_atomic_store((gu32*)(ws + WS_EPOCH), tag, RLX_AGENT);
  }
}

extern "C" void kernel_launch(void* const* d_in, const int* in_sizes, int n_in, void* d_out, int out_size, void* d_ws, size_t ws_size,
                              hipStream_t stream) {
  (void)in_sizes; (void)n_in; (void)out_size; (void)ws_size;
  fused_decode_kernel<<<NWG, NTHR, 0, stream>>>((const unsigned short*)d_in[0], (const unsigned short*)d_in[1], (const unsigned short*)d_in[2],
                                                (const unsigned short*)d_in[3], (const unsigned short*)d_in[4], (const unsigned short*)d_in[6],
                                                (const unsigned short*)d_in[7], (const unsigned short*)d_in[8], (unsigned short*)d_out,
                                                (unsigned char*)d_ws);
}
```

```cpp
#include <hip/hip_runtime.h>
#include <hip/hip_bf16.h>
#include <stdint.h>

#define GAS __attribute__((address_space(1)))
#define LAS __attribute__((address_space(3)))
#define RLX_AGENT __ATOMIC_RELAXED, __HIP_MEMORY_SCOPE_AGENT
typedef unsigned v4u __attribute__((ext_vector_type(4)));
typedef unsigned v2u __attribute__((ext_vector_type(2)));
typedef float f32x4 __attribute__((ext_vector_type(4)));
typedef float f32x2 __attribute__((ext_vector_type(2)));
typedef short bf16x8 __attribute__((ext_vector_type(8)));
typedef short s16x4 __attribute__((ext_vector_type(4)));
typedef __bf16 bf16x2 __attribute__((ext_vector_type(2)));
typedef unsigned long long u64;
typedef GAS u64 gu64;
typedef GAS unsigned gu32;

namespace {
template <int N> struct IC { static constexpr int value = N; };
constexpr int NB = 8, DM = 512, HQ = 32, HKV = 8, SK = 8192, DH = 128, FF = 1792;
constexpr int NWG = 256, NTHR = 512;
constexpr float SCALE = 0.08838834764831845f;
constexpr float L2E = 1.4426950408889634f;
constexpr unsigned SPIN_CAP = 1u << 18;
constexpr int AUX_NT = 2, AUX_SC1 = 16, RS3 = 0x00020000;

constexpr int WS_EPOCH = 0;
constexpr int WS_TS = 256;
constexpr int WS_QP = 4096;
constexpr int WS_GP = WS_QP + 2 * NB * 4096 * 8;
constexpr int WS_HG = WS_GP + 2 * 2 * NB * FF * 8;
constexpr int WS_MG = WS_HG + NB * 896 * 8;
constexpr int WS_PG = WS_MG + 64 * 16 * 8;
constexpr int WS_SG = WS_PG + NWG * 256 * 8;
constexpr int WS_DG = WS_SG + HQ * NB * DM * 8;
constexpr int WS_END = WS_DG + HQ * NB * DM * 8;

constexpr int XN_STR = 1040;
constexpr int L_XN = 0;
constexpr int L_T0 = 8448;
constexpr int L_T1 = L_T0 + 16384;
constexpr int QS_STR = 272;
constexpr int L_QS = L_T1 + 16384;
constexpr int L_MRG = L_QS + 1152;
constexpr int L_ML = L_MRG + 16384;
constexpr int L_O = L_ML + 256;
constexpr int L_WD = L_O + 8 * QS_STR;
constexpr int L_FLAG = L_WD + 8192;
constexpr int L_WO = L_FLAG + 64;
constexpr int L_END = L_WO + 16384;

__device__ __forceinline__ f32x4 mfma(v4u a, v4u b, f32x4 c) {
  return __builtin_amdgcn_mfma_f32_16x16x32_bf16(__builtin_bit_cast(bf16x8, a), __builtin_bit_cast(bf16x8, b), c, 0, 0, 0);
}
__device__ __forceinline__ unsigned cvtpk(float lo, float hi) {
  f32x2 v = {lo, hi};
  return __builtin_bit_cast(unsigned, __builtin_convertvector(v, bf16x2));
}
__device__ __forceinline__ float bflo(unsigned u) { return __builtin_bit_cast(float, u << 16); }
__device__ __forceinline__ float bfhi(unsigned u) { return __builtin_bit_cast(float, u & 0xffff0000u); }
__device__ __forceinline__ float rbf(float x) { return bflo(cvtpk(x, x)); }
__device__ __forceinline__ float u2f(unsigned u) { return __builtin_bit_cast(float, u); }
template <int CTRL> __device__ __forceinline__ float dpp(float x) {
  return __builtin_bit_cast(float, __builtin_amdgcn_mov_dpp(__builtin_bit_cast(int, x), CTRL, 0xf, 0xf, true));
}
constexpr int ROR1 = 0x121, ROR2 = 0x122, ROR4 = 0x124, ROR8 = 0x128;
__device__ __forceinline__ float xrow_max(float x) {
  auto s = __builtin_amdgcn_permlane16_swap(__float_as_uint(x), __float_as_uint(x), false, false);
  x = fmaxf(__uint_as_float(s[0]), __uint_as_float(s[1]));
  auto t = __builtin_amdgcn_permlane32_swap(__float_as_uint(x), __float_as_uint(x), false, false);
  return fmaxf(__uint_as_float(t[0]), __uint_as_float(t[1]));
}
__device__ __forceinline__ float xrow_sum(float x) {
  auto s = __builtin_amdgcn_permlane16_swap(__float_as_uint(x), __float_as_uint(x), false, false);
  x = __uint_as_float(s[0]) + __uint_as_float(s[1]);
  auto t = __builtin_amdgcn_permlane32_swap(__float_as_uint(x), __float_as_uint(x), false, false);
  return __uint_as_float(t[0]) + __uint_as_float(t[1]);
}
__device__ __forceinline__ float row_sum(float x) {
  x += dpp<ROR8>(x); x += dpp<ROR4>(x); x += dpp<ROR2>(x); x += dpp<ROR1>(x); return x;
}
__device__ __forceinline__ float wave_sum(float x) { return xrow_sum(row_sum(x)); }
__device__ __forceinline__ v4u ldnt(const void* p) { return __builtin_nontemporal_load((const GAS v4u*)p); }
__device__ __forceinline__ void st_gran(gu64* g, unsigned tag, unsigned v) {
  __hip_atomic_store(g, ((u64)tag << 32) | (u64)v, RLX_AGENT);
}
__device__ __forceinline__ u64 ld_gran(gu64* g) { return __hip_atomic_load(g, RLX_AGENT); }
__device__ __forceinline__ void publish_tile(__amdgpu_buffer_rsrc_t rs, int off, f32x4 a, unsigned tag, int c) {
  const bool hi = c >= 8;
  const v4u v = {__float_as_uint(hi ? a[2] : a[0]), tag, __float_as_uint(hi ? a[3] : a[1]), tag};
  __builtin_amdgcn_raw_buffer_store_b128(v, rs, off + (hi ? 16 : 0), 0, AUX_SC1);
}
__device__ __forceinline__ v4u tile_frag(LAS unsigned char* wa, int ks) {
  const v2u lo = __builtin_bit_cast(v2u, __builtin_amdgcn_ds_read_tr16_b64_v4i16((LAS s16x4*)(wa + ks * 4096)));
  const v2u hi = __builtin_bit_cast(v2u, __builtin_amdgcn_ds_read_tr16_b64_v4i16((LAS s16x4*)(wa + ks * 4096 + 512)));
  return v4u{lo.x, lo.y, hi.x, hi.y};
}
__device__ __forceinline__ void lds_arrive(LAS unsigned* p, int lane) {
  __builtin_amdgcn_fence(__ATOMIC_RELEASE, "workgroup");
  if (lane == 0) __hip_atomic_fetch_add(p, 1u, __ATOMIC_RELAXED, __HIP_MEMORY_SCOPE_WORKGROUP);
}
__device__ __forceinline__ void lds_wait(LAS unsigned* p, unsigned n) {
  while (__hip_atomic_load(p, __ATOMIC_RELAXED, __HIP_MEMORY_SCOPE_WORKGROUP) < n) __builtin_amdgcn_s_sleep(1);
  __builtin_amdgcn_fence(__ATOMIC_ACQUIRE, "workgroup");
}
__device__ __forceinline__ v4u layernorm_row(v4u xv) {
  float xf[8];
#pragma unroll
  for (int k = 0; k < 4; ++k) { xf[2 * k] = bflo(xv[k]); xf[2 * k + 1] = bfhi(xv[k]); }
  float s = 0.f;
#pragma unroll
  for (int k = 0; k < 8; ++k) s += xf[k];
  const float mean = wave_sum(s) * (1.0f / DM);
  float q2 = 0.f;
#pragma unroll
  for (int k = 0; k < 8; ++k) { const float d = xf[k] - mean; q2 = fmaf(d, d, q2); }
  const float var = wave_sum(q2) * (1.0f / DM);
  const float mb = rbf(mean), inv = __builtin_amdgcn_rcpf(rbf(__builtin_amdgcn_sqrtf(rbf(rbf(var) + 1e-5f))));
  v4u o;
#pragma unroll
  for (int k = 0; k < 4; ++k) { const unsigned dd = cvtpk(xf[2 * k] - mb, xf[2 * k + 1] - mb); o[k] = cvtpk(bflo(dd) * inv, bfhi(dd) * inv); }
  return o;
}
}

#define PACE 3850
#define SLOT_GAP 4
#define SLOT_GAP2 1
#define Q_AT 480
#define WO_D0 60
#define WO_STEP 10
#define S2_DELAY 155
#define S4_DELAY 310
#define LATE2 20
#define LATE4 50
#define FINAL_WAVE 5
#define NOW() ((unsigned)__builtin_amdgcn_s_memrealtime())
#define SPIN_PAUSE() do { __builtin_amdgcn_s_sleep(1); asm volatile("" ::: "memory"); } while (0)

__global__ void __launch_bounds__(NTHR, 2)
fused_decode_kernel(const unsigned short* __restrict__ x, const unsigned short* __restrict__ Kc, const unsigned short* __restrict__ Vc,
                    const unsigned short* __restrict__ wq, const unsigned short* __restrict__ wo, const unsigned short* __restrict__ wg,
                    const unsigned short* __restrict__ wu, const unsigned short* __restrict__ wd, unsigned short* __restrict__ out,
                    unsigned char* ws) {
  __shared__ __attribute__((aligned(16))) unsigned char lds_raw[L_END];
  LAS unsigned char* L = (LAS unsigned char*)lds_raw;
  const int tid = threadIdx.x, lane = tid & 63, w = __builtin_amdgcn_readfirstlane(tid >> 6);
  const int g = lane >> 4, c = lane & 15;
  const int j = blockIdx.x, xc = j & 7, idx = j >> 3;
  const int b = idx >> 2, sp = idx & 3;
  const int pair = xc * HKV + (idx >> 2);
  const int h2 = 4 * xc + (idx & 3), cq = idx >> 2;
  const int cb = 4 * xc + (idx & 3);
  const bool isq = (idx & 1) == 0;
  const int fu = xc * 14 + (idx >> 1), fmat = fu & 1;
  const bool has_tile = isq || (idx >> 1) < 14;
  const int tkc = isq ? (idx >> 1) & 1 : (fu >> 1) & 1;
  const int tcol = isq ? 512 * xc + 64 * (idx >> 2) : 64 * (fu >> 2);
  const int tld = isq ? HQ * DH : FF;
  const int q4 = c >> 2, p4 = lane & 3, mt = w & 3;
  const __amdgpu_buffer_rsrc_t rws = __builtin_amdgcn_make_buffer_rsrc((void*)ws, 0, WS_END, RS3);

  const unsigned tstart = NOW();
  unsigned epoch = *(const __attribute__((address_space(4))) unsigned*)(ws + WS_EPOCH);
  LAS unsigned* flags = (LAS unsigned*)(L + L_FLAG);
  if (tid < 16) flags[tid] = 0u;
  __syncthreads();

  if (w < 4) {
    const unsigned tag = epoch + 1u;
    const v4u xv0 = *(const GAS v4u*)(x + (2 * w) * DM + lane * 8), xv1 = *(const GAS v4u*)(x + (2 * w + 1) * DM + lane * 8);
    const __amdgpu_buffer_rsrc_t rt = __builtin_amdgcn_make_buffer_rsrc((void*)(isq ? wq : (fmat ? wu : wg)), 0, has_tile ? DM * tld * 2 : 0, RS3);
    const int toff = ((256 * tkc + 64 * w + (lane >> 3)) * tld + tcol + (lane & 7) * 8) * 2;
    v4u ta[8];
#pragma unroll
    for (int u = 0; u < 8; ++u) ta[u] = __builtin_amdgcn_raw_buffer_load_b128(rt, toff, u * 8 * tld * 2, AUX_NT);
    unsigned ptv;
    {
      const size_t kvb = ((size_t)pair * SK + sp * 256) * (DH * 2);
      const int pl = (w == 3 && lane < 19) ? lane : 8;
      const char* pa = pl < 8 ? (const char*)Vc + kvb + pl * (1024 * 256) : pl < 16 ? (const char*)Kc + kvb + (pl - 8) * (1024 * 256)
                     : pl == 16 ? (const char*)(wo + (size_t)(128 * h2) * DM + 64 * cq) : pl == 17 ? (const char*)(wd + (size_t)(56 * h2) * DM + 64 * cq) : (const char*)(out + b * DM + 16 * cb);
      ptv = __builtin_nontemporal_load((const GAS unsigned*)pa);
    }
    __builtin_amdgcn_sched_barrier(0);
    const v4u o0 = layernorm_row(xv0), o1 = layernorm_row(xv1);
    *(LAS v4u*)(L + L_XN + (2 * w) * XN_STR + lane * 16) = o0;
    *(LAS v4u*)(L + L_XN + (2 * w + 1) * XN_STR + lane * 16) = o1;
#pragma unroll
    for (int u = 0; u < 8; ++u) *(LAS v4u*)(L + L_T0 + w * 8192 + u * 1024 + lane * 16) = ta[u];
    lds_arrive(flags + 1, lane);
    lds_wait(flags + 1, 4u);
    {
      LAS unsigned char* wa = L + L_T0 + (8 * g + q4) * 128 + mt * 32 + 8 * p4;
      LAS unsigned char* xb = L + L_XN + (c & 7) * XN_STR + tkc * 512 + g * 16;
      f32x4 a0 = {0.f, 0.f, 0.f, 0.f}, a1 = {0.f, 0.f, 0.f, 0.f};
#pragma unroll
      for (int ks = 0; ks < 8; ks += 2) {
        a0 = mfma(tile_frag(wa, ks), *(LAS v4u*)(xb + ks * 64), a0);
        a1 = mfma(tile_frag(wa, ks + 1), *(LAS v4u*)(xb + ks * 64 + 64), a1);
      }
      a0 += a1;
      const int dst = isq ? WS_QP + ((tkc * NB + (c & 7)) * 4096) * 8 : WS_GP + (((fmat * 2 + tkc) * NB + (c & 7)) * FF) * 8;
      if (has_tile) publish_tile(rws, dst + (tcol + 16 * mt + 4 * g) * 8, a0, tag, c);
    }
    asm volatile("" :: "v"(ptv));
    if (w == 0) {
      int qo = WS_QP + (pair * 512 + 2 * lane) * 8;
      v4u qr[2][4];
      u64 tsg;
      while ((int)(NOW() - tstart) < Q_AT) __builtin_amdgcn_s_sleep(1);
      for (unsigned spins = 0;; ++spins) {
        asm volatile("" : "+v"(qo));
#pragma unroll
        for (int kc = 0; kc < 2; ++kc)
#pragma unroll
          for (int hh = 0; hh < 4; ++hh) qr[kc][hh] = __builtin_amdgcn_raw_buffer_load_b128(rws, qo, (kc * NB * 4096 + hh * 128) * 8, AUX_SC1);
        tsg = ld_gran((gu64*)(ws + WS_TS) + (lane & 7));
        bool ok = true;
#pragma unroll
        for (int kc = 0; kc < 2; ++kc)
#pragma unroll
          for (int hh = 0; hh < 4; ++hh) ok &= (qr[kc][hh][1] == tag) & (qr[kc][hh][3] == tag);
        if (__all(ok) || spins > SPIN_CAP) break;
        SPIN_PAUSE();
      }
#pragma unroll
      for (int hh = 0; hh < 4; ++hh)
        *(LAS unsigned*)(L + L_QS + hh * QS_STR + lane * 4) = cvtpk(u2f(qr[0][hh][0]) + u2f(qr[1][hh][0]), u2f(qr[0][hh][2]) + u2f(qr[1][hh][2]));
      {
        int dt = ((unsigned)(tsg >> 32) == tag) ? (int)((unsigned)tsg - tstart) : 0;
        dt = min(max(dt, -400), 400);
        dt = min(dt, __builtin_amdgcn_mov_dpp(dt, 0xB1, 0xf, 0xf, true)); dt = min(dt, __builtin_amdgcn_mov_dpp(dt, 0x4E, 0xf, 0xf, true)); dt = min(dt, __builtin_amdgcn_mov_dpp(dt, 0x141, 0xf, 0xf, true));
        if (lane == 0) flags[4] = tstart + (unsigned)dt;
      }
      lds_arrive(flags, lane);
    }
  }
  __builtin_amdgcn_sched_barrier(0);

  const size_t kvbase = ((size_t)pair * SK + sp * 256) * (DH * 2);
  const int tn0_ = (w >= 4) ? (w - 4) : (16 + 3 - w), tn1_ = 32 + ((w >= 4) ? (w - 4) : (7 - w));
#define TILE_N(t) ((t) < 4 ? tn0_ + 4 * (t) : tn1_ + 8 * ((t) - 4))
#define TILE_OFF(t) ((TILE_N(t) >> 3) * (1024 * 256) + (TILE_N(t) & 7) * 8192)
  const __amdgpu_buffer_rsrc_t rk = __builtin_amdgcn_make_buffer_rsrc((void*)((const char*)Kc + kvbase), 0, 0x7fffffff, RS3);
  const __amdgpu_buffer_rsrc_t rv = __builtin_amdgcn_make_buffer_rsrc((void*)((const char*)Vc + kvbase), 0, 0x7fffffff, RS3);
  const int koff = (c >> 1) * 256 + (c & 1) * 64 + g * 16;
  const int voff = g * 512 + c * 16;
  v4u buf[4][8];
#define ISSUE(gi) { _Pragma("unroll") for (int u = 0; u < 8; ++u) buf[(gi) & 3][u] = (gi) < 8 \
    ? __builtin_amdgcn_raw_buffer_load_b128(rk, koff + ((u >> 1) & 1) * 2048 + (u & 1) * 128, TILE_OFF((gi) & 7) + (u >> 2) * 4096, AUX_NT) \
    : __builtin_amdgcn_raw_buffer_load_b128(rv, voff + ((u >> 1) & 1) * 2048 + (u & 1) * 256, TILE_OFF((gi) & 7) + (u >> 2) * 4096, AUX_NT); __builtin_amdgcn_sched_barrier(0); }
  unsigned tbase = tstart;
  const unsigned phase = (unsigned)((((xc + (idx >> 2)) & 7) * PACE) >> 10);
#define SLOT_OF(gi) (TILE_N((gi) & 7) + ((gi) >= 8 ? 64 : 0) + ((gi) >= 4 || w < 4 ? SLOT_GAP : 0) + ((gi) >= 6 ? SLOT_GAP2 : 0))
#define PACE_WAIT(gi) { const unsigned due_ = tbase + phase + (unsigned)((SLOT_OF(gi) * PACE) >> 7); while ((int)(NOW() - due_) < 0) __builtin_amdgcn_s_sleep(1); __builtin_amdgcn_sched_barrier(0); }
  PACE_WAIT(0) ISSUE(0)
  asm volatile("" : "+s"(epoch));
  const unsigned tag = epoch + 1u;
  if (w == 4 && idx == 0 && lane == 0) st_gran((gu64*)(ws + WS_TS) + xc, tag, tstart);
  __builtin_amdgcn_sched_barrier(0);
  PACE_WAIT(1) ISSUE(1) PACE_WAIT(2) ISSUE(2) PACE_WAIT(3) ISSUE(3)
  __builtin_amdgcn_sched_barrier(0);
  lds_wait(flags, 1u);
  tbase = __builtin_amdgcn_readfirstlane(flags[4]);

  v4u qf[2];
#pragma unroll
  for (int hf = 0; hf < 2; ++hf) qf[hf] = *(LAS v4u*)(L + L_QS + (c & 3) * QS_STR + hf * 128 + ((c >> 2) & 1) * 64 + g * 16);
  const bool up = (c >> 2) & 1;
  f32x4 acc[8];
#pragma unroll
  for (int t = 0; t < 8; ++t) acc[t] = f32x4{0.f, 0.f, 0.f, 0.f};
  float m = -INFINITY, fw = 0.f;
  v4u sp_[8];

  auto qk = [&](const v4u (&kt)[8], v4u& sc) __attribute__((always_inline)) {
#pragma unroll
    for (int G = 0; G < 4; ++G) {
      f32x4 dg = mfma(kt[2 * G], qf[0], f32x4{0.f, 0.f, 0.f, 0.f});
      dg = mfma(kt[2 * G + 1], qf[1], dg);
      const float e0 = up ? dg[1] : dg[0], e1 = up ? dg[3] : dg[2];
      const float s0 = e0 + dpp<ROR4>(e0), s1 = e1 + dpp<ROR4>(e1);
      const unsigned a0 = cvtpk(s0, s1);
      sc[G] = cvtpk(bflo(a0) * SCALE, bfhi(a0) * SCALE);
      m = fmaxf(m, fmaxf(bflo(sc[G]), bfhi(sc[G])));
    }
    asm volatile("" : "+v"(sc), "+v"(m));
    __builtin_amdgcn_sched_barrier(0);
  };
  auto pv = [&](const v4u (&vt)[8], const v4u& pf) __attribute__((always_inline)) {
#pragma unroll
    for (int t = 0; t < 8; ++t) {
      const unsigned sel = (t & 1) ? 0x07060302u : 0x05040100u;
      v4u a;
#pragma unroll
      for (int mm = 0; mm < 4; ++mm) a[mm] = __builtin_amdgcn_perm(vt[2 * mm + 1][t >> 1], vt[2 * mm][t >> 1], sel);
      acc[t] = mfma(a, pf, acc[t]);
    }
#pragma unroll
    for (int t = 0; t < 8; ++t) asm volatile("" : "+v"(acc[t]));
    __builtin_amdgcn_sched_barrier(0);
  };
#define CONSUME(gi) { if ((gi) < 8) qk(buf[(gi) & 3], sp_[(gi) & 7]); else pv(buf[(gi) & 3], sp_[(gi) & 7]); }

  v4u wt, af[4];
  float gate0 = 0.f, gate1 = 0.f;
  unsigned xfin = 0u;
  const int fo = ((lane >> 3) * NB + b) * DM + 16 * cb + 2 * (lane & 7);
  const int ord = (w >= 4) ? (w - 4) : (7 - w);

  CONSUME(0)
  auto step = [&](auto GI) __attribute__((always_inline)) {
    constexpr int gi = decltype(GI)::value;
    PACE_WAIT(gi) ISSUE(gi)
    if (gi < 15) CONSUME(gi - 3)
    if (gi == 14) CONSUME(12)
    if (gi >= 14) CONSUME(gi - 1)
    if (gi == 10) {
      m = xrow_max(m);
      const float nm = -m * L2E;
      float l0 = 0.f, l1 = 0.f;
#pragma unroll
      for (int t = 0; t < 8; ++t)
#pragma unroll
        for (int G = 0; G < 4; ++G) {
          const float p0 = __builtin_amdgcn_exp2f(fmaf(bflo(sp_[t][G]), L2E, nm)), p1 = __builtin_amdgcn_exp2f(fmaf(bfhi(sp_[t][G]), L2E, nm));
          l0 += p0; l1 += p1;
          sp_[t][G] = cvtpk(p0, p1);
        }
      const float l = xrow_sum(l0 + l1);
      if (lane < 4) *(LAS f32x2*)(L + L_ML + (w * 4 + lane) * 8) = f32x2{m, l};
      lds_arrive(flags + 2, lane);
    }
    if (gi == 5 && w == 1) {
      int go = WS_GP + (b * FF + 56 * h2 + 2 * (lane < 28 ? lane : 27)) * 8;
      v4u gr[2][2];
      for (unsigned spins = 0;; ++spins) {
        asm volatile("" : "+v"(go));
#pragma unroll
        for (int mm = 0; mm < 2; ++mm)
#pragma unroll
          for (int kc = 0; kc < 2; ++kc) gr[mm][kc] = __builtin_amdgcn_raw_buffer_load_b128(rws, go, ((mm * 2 + kc) * NB * FF) * 8, AUX_SC1);
        bool ok = true;
#pragma unroll
        for (int mm = 0; mm < 2; ++mm)
#pragma unroll
          for (int kc = 0; kc < 2; ++kc) ok &= (gr[mm][kc][1] == tag) & (gr[mm][kc][3] == tag);
        if (__all(ok) || spins > SPIN_CAP) break;
        SPIN_PAUSE();
      }
      const float g0 = rbf(u2f(gr[0][0][0]) + u2f(gr[0][1][0])), g1 = rbf(u2f(gr[0][0][2]) + u2f(gr[0][1][2]));
      const float u0 = rbf(u2f(gr[1][0][0]) + u2f(gr[1][1][0])), u1 = rbf(u2f(gr[1][0][2]) + u2f(gr[1][1][2]));
      const float h0 = (g0 > 0.f ? g0 : 0.f * g0) * u0, h1 = (g1 > 0.f ? g1 : 0.f * g1) * u1;
      if (lane < 28) st_gran((gu64*)(ws + WS_HG) + b * 896 + 28 * h2 + lane, tag, cvtpk(h0, h1));
    }
    if (gi == 5)
      wt = ldnt(wd + (size_t)(56 * h2 + 8 * (w < 7 ? w : 6) + (lane >> 3)) * DM + 64 * cq + (lane & 7) * 8);
    if (gi == 7) {
      *(LAS v4u*)(L + L_WD + w * 1024 + lane * 16) = wt;
      lds_arrive(flags + 3, lane);
    }
    if (gi == 11 && w == 0) {
      lds_wait(flags + 2, 8u);
      f32x2 ml[8];
      float M = -INFINITY, Ls = 0.f;
#pragma unroll
      for (int k = 0; k < 8; ++k) { ml[k] = *(LAS f32x2*)(L + L_ML + (k * 4 + (lane & 3)) * 8); M = fmaxf(M, ml[k].x); }
#pragma unroll
      for (int k = 0; k < 8; ++k) Ls = fmaf(__builtin_amdgcn_exp2f((ml[k].x - M) * L2E), ml[k].y, Ls);
      if (lane < 4) st_gran((gu64*)(ws + WS_MG) + (pair * 4 + sp) * 4 + lane, tag, __float_as_uint(fmaf(M, L2E, __builtin_amdgcn_logf(Ls))));
    }
    if (gi == 11 && w >= 4) {
      int ho = WS_HG + ((c & 7) * 896 + 28 * h2 + 4 * g) * 8, ho1 = ho + (g < 3 ? 128 : 0);
      v4u hgr[4];
      for (unsigned spins = 0;; ++spins) {
        asm volatile("" : "+v"(ho), "+v"(ho1));
        hgr[0] = __builtin_amdgcn_raw_buffer_load_b128(rws, ho, 0, AUX_SC1); hgr[1] = __builtin_amdgcn_raw_buffer_load_b128(rws, ho, 16, AUX_SC1);
        hgr[2] = __builtin_amdgcn_raw_buffer_load_b128(rws, ho1, 0, AUX_SC1); hgr[3] = __builtin_amdgcn_raw_buffer_load_b128(rws, ho1, 16, AUX_SC1);
        bool ok = true;
#pragma unroll
        for (int k = 0; k < 4; ++k) ok &= (hgr[k][1] == tag) & (hgr[k][3] == tag);
        if (__all(ok) || spins > SPIN_CAP) break;
        SPIN_PAUSE();
      }
      lds_wait(flags + 3, 8u);
      LAS unsigned char* wa = L + L_WD + (8 * g + q4) * 128 + mt * 32 + 8 * p4;
      const v4u b0 = {hgr[0][0], hgr[0][2], hgr[1][0], hgr[1][2]};
      v4u b1 = {hgr[2][0], hgr[2][2], hgr[3][0], hgr[3][2]};
      if (g == 3) b1 = v4u{0u, 0u, 0u, 0u};
      f32x4 dd = mfma(tile_frag(wa, 0), b0, f32x4{0.f, 0.f, 0.f, 0.f});
      dd = mfma(tile_frag(wa, 1), b1, dd);
      publish_tile(rws, WS_DG + ((h2 * NB + (c & 7)) * DM + 64 * cq + 16 * mt + 4 * g) * 8, dd, tag, c);
    }
    if (gi == 13) {
      gu64* mg = (gu64*)(ws + WS_MG) + pair * 16 + c;
      u64 lg;
      for (unsigned spins = 0;; ++spins) {
        lg = ld_gran(mg);
        if (__all((unsigned)(lg >> 32) == tag) || spins > SPIN_CAP) break;
        SPIN_PAUSE();
      }
      const float ls = u2f((unsigned)lg);
      float mx = fmaxf(ls, dpp<ROR4>(ls)); mx = fmaxf(mx, dpp<ROR8>(mx));
      const float e = __builtin_amdgcn_exp2f(ls - mx), e2 = e + dpp<ROR8>(e), se = e2 + dpp<ROR4>(e2);
      fw = __builtin_amdgcn_exp2f(fmaf(m, L2E, -mx)) * __builtin_amdgcn_rcpf(se);
      asm volatile("" : "+v"(fw));
    }
    if (gi == 14 && w == FINAL_WAVE) {
      int dof = WS_DG + fo * 8;
      v4u dr[4];
      xfin = *(const GAS unsigned*)(x + b * DM + 16 * cb + 2 * (lane & 7));
      for (unsigned spins = 0;; ++spins) {
        asm volatile("" : "+v"(dof));
#pragma unroll
        for (int k = 0; k < 4; ++k) dr[k] = __builtin_amdgcn_raw_buffer_load_b128(rws, dof, k * 8 * NB * DM * 8, AUX_SC1);
        bool ok = true;
#pragma unroll
        for (int k = 0; k < 4; ++k) ok &= (dr[k][1] == tag) & (dr[k][3] == tag);
        if (__all(ok) || spins > SPIN_CAP) break;
        SPIN_PAUSE();
      }
      float d0 = (u2f(dr[0][0]) + u2f(dr[1][0])) + (u2f(dr[2][0]) + u2f(dr[3][0])), d1 = (u2f(dr[0][2]) + u2f(dr[1][2])) + (u2f(dr[2][2]) + u2f(dr[3][2]));
      d0 += dpp<ROR8>(d0); d1 += dpp<ROR8>(d1);
      d0 = rbf(xrow_sum(d0)); d1 = rbf(xrow_sum(d1));
      gate0 = rbf(d0 / (1.0f + __expf(-d0))); gate1 = rbf(d1 / (1.0f + __expf(-d1)));
    }
    __builtin_amdgcn_sched_barrier(0);
  };
  step(IC<4>{}); step(IC<5>{}); step(IC<6>{}); step(IC<7>{}); step(IC<8>{}); step(IC<9>{});
  step(IC<10>{}); step(IC<11>{}); step(IC<12>{}); step(IC<13>{}); step(IC<14>{}); step(IC<15>{});
  const int u = c >> 2;
  LAS unsigned char* rec = L + L_MRG + (c & 3) * 512 + g * 128 + u * 16;
#define PICKA(k) ((u & 1) ? ((u & 2) ? acc[4 + (k)][1] : acc[4 + (k)][0]) : ((u & 2) ? acc[k][1] : acc[k][0]))
#define PICKB(k) ((u & 1) ? ((u & 2) ? acc[4 + (k)][3] : acc[4 + (k)][2]) : ((u & 2) ? acc[k][3] : acc[k][2]))
  f32x4 xA = {0.f, 0.f, 0.f, 0.f}, xB = {0.f, 0.f, 0.f, 0.f};
  if (ord == 7) {
#pragma unroll
    for (int k = 0; k < 7; ++k) {
      lds_wait(flags + 8 + k, 1u);
      xA += *(LAS f32x4*)(rec + k * 2048); xB += *(LAS f32x4*)(rec + k * 2048 + 64);
    }
    asm volatile("" : "+v"(xA), "+v"(xB));
  }
  __builtin_amdgcn_sched_barrier(0);
  CONSUME(15)
  const f32x4 pA = f32x4{PICKA(0), PICKA(1), PICKA(2), PICKA(3)} * fw + xA, pB = f32x4{PICKB(0), PICKB(1), PICKB(2), PICKB(3)} * fw + xB;
  if (ord == 7) {
    const int po = WS_PG + ((pair * 4 + sp) * 256 + (c & 3) * 64) * 8 + g * 128 + u * 16;
    __builtin_amdgcn_raw_buffer_store_b128(v4u{cvtpk(pA[0], pA[1]), tag, cvtpk(pA[2], pA[3]), tag}, rws, po, 0, AUX_SC1);
    __builtin_amdgcn_raw_buffer_store_b128(v4u{cvtpk(pB[0], pB[1]), tag, cvtpk(pB[2], pB[3]), tag}, rws, po + 64, 0, AUX_SC1);
    lds_arrive(flags + 15, lane);
  } else {
    *(LAS f32x4*)(rec + ord * 2048) = pA;
    *(LAS f32x4*)(rec + ord * 2048 + 64) = pB;
    lds_arrive(flags + 8 + ord, lane);
  }
  const unsigned tpub0 = tbase + (unsigned)(((127 + SLOT_GAP + SLOT_GAP2) * PACE) >> 7);
  v4u wl[4];
  {
    if (w >= 4) while ((int)(NOW() - tpub0) < WO_D0 + mt * WO_STEP) __builtin_amdgcn_s_sleep(1);
    const __amdgpu_buffer_rsrc_t rwo = __builtin_amdgcn_make_buffer_rsrc((void*)wo, 0, w >= 4 ? HQ * DH * DM * 2 : 0, RS3);
    const int wof = ((128 * h2 + 32 * mt + (lane >> 3)) * DM + 64 * cq + (lane & 7) * 8) * 2;
#pragma unroll
    for (int k = 0; k < 4; ++k) wl[k] = __builtin_amdgcn_raw_buffer_load_b128(rwo, wof, k * 8 * DM * 2, AUX_NT);
    __builtin_amdgcn_sched_barrier(0);
  }
  {
    const int hf = lane >> 5;
    int po = WS_PG + (((w * HKV + xc) * 4 + hf) * 256 + (h2 & 3) * 64 + 2 * (lane & 31)) * 8;
    v4u gr[2];
    lds_wait(flags + 15, 1u);
    {
      const int t2 = max((int)(S2_DELAY), (int)(NOW() - tpub0) + LATE2);
      while ((int)(NOW() - tpub0) < t2) {}
    }
    gr[0] = __builtin_amdgcn_raw_buffer_load_b128(rws, po, 0, AUX_SC1);
    gr[1] = __builtin_amdgcn_raw_buffer_load_b128(rws, po, 2 * 256 * 8, AUX_SC1);
    if (w >= 4) {
      __builtin_amdgcn_sched_barrier(0);
#pragma unroll
      for (int k = 0; k < 4; ++k) *(LAS v4u*)(L + L_WO + mt * 4096 + k * 1024 + lane * 16) = wl[k];
      lds_arrive(flags + 5, lane);
      __builtin_amdgcn_sched_barrier(0);
    }
    for (unsigned spins = 0;; ++spins) {
      const bool k0 = (gr[0][1] == tag) & (gr[0][3] == tag), k1 = (gr[1][1] == tag) & (gr[1][3] == tag);
      if (__all(k0 & k1) || spins > SPIN_CAP) break;
      asm volatile("" : "+v"(po) :: "memory");
      if (!k0) gr[0] = __builtin_amdgcn_raw_buffer_load_b128(rws, po, 0, AUX_SC1);
      if (!k1) gr[1] = __builtin_amdgcn_raw_buffer_load_b128(rws, po, 2 * 256 * 8, AUX_SC1);
    }
    const float o0 = bflo(gr[0][0]) + bflo(gr[1][0]), o1 = bfhi(gr[0][0]) + bfhi(gr[1][0]), o2 = bflo(gr[0][2]) + bflo(gr[1][2]), o3 = bfhi(gr[0][2]) + bfhi(gr[1][2]);
    auto s01 = __builtin_amdgcn_permlane32_swap(__float_as_uint(o0), __float_as_uint(o2), false, false);
    auto s23 = __builtin_amdgcn_permlane32_swap(__float_as_uint(o1), __float_as_uint(o3), false, false);
    *(LAS unsigned*)(L + L_O + w * QS_STR + (lane & 31) * 8 + hf * 4) = cvtpk(u2f(s01[0]) + u2f(s01[1]), u2f(s23[0]) + u2f(s23[1]));
  }
  if (w < 4) {
    lds_wait(flags + 5, 4u);
    LAS unsigned char* wa = L + L_WO + (8 * g + q4) * 128 + mt * 32 + 8 * p4;
#pragma unroll
    for (int ks = 0; ks < 4; ++ks) { af[ks] = tile_frag(wa, ks); asm volatile("" : "+v"(af[ks])); }
  }
  __syncthreads();
  if (w < 4) {
    LAS unsigned char* ob = L + L_O + (c & 7) * QS_STR + g * 16;
    f32x4 s0 = mfma(af[0], *(LAS v4u*)(ob), f32x4{0.f, 0.f, 0.f, 0.f});
    f32x4 s1 = mfma(af[1], *(LAS v4u*)(ob + 64), f32x4{0.f, 0.f, 0.f, 0.f});
    s0 = mfma(af[2], *(LAS v4u*)(ob + 128), s0);
    s1 = mfma(af[3], *(LAS v4u*)(ob + 192), s1);
    s0 += s1;
    publish_tile(rws, WS_SG + ((h2 * NB + (c & 7)) * DM + 64 * cq + 16 * mt + 4 * g) * 8, s0, tag, c);
  }
  if (w == FINAL_WAVE) {
    int sof = WS_SG + fo * 8;
    v4u sr[4];
    {
      const int t4 = max((int)(S4_DELAY), (int)(NOW() - tpub0) + LATE4);
      while ((int)(NOW() - tpub0) < t4) {}
    }
#pragma unroll
    for (int k = 0; k < 4; ++k) sr[k] = __builtin_amdgcn_raw_buffer_load_b128(rws, sof, k * 8 * NB * DM * 8, AUX_SC1);
    for (unsigned spins = 0;; ++spins) {
      const bool k0 = (sr[0][1] == tag) & (sr[0][3] == tag), k1 = (sr[1][1] == tag) & (sr[1][3] == tag),
                 k2 = (sr[2][1] == tag) & (sr[2][3] == tag), k3 = (sr[3][1] == tag) & (sr[3][3] == tag);
      if (__all(k0 & k1 & k2 & k3) || spins > SPIN_CAP) break;
      asm volatile("" : "+v"(sof) :: "memory");
      if (!k0) sr[0] = __builtin_amdgcn_raw_buffer_load_b128(rws, sof, 0, AUX_SC1);
      if (!k1) sr[1] = __builtin_amdgcn_raw_buffer_load_b128(rws, sof, 8 * NB * DM * 8, AUX_SC1);
      if (!k2) sr[2] = __builtin_amdgcn_raw_buffer_load_b128(rws, sof, 2 * 8 * NB * DM * 8, AUX_SC1);
      if (!k3) sr[3] = __builtin_amdgcn_raw_buffer_load_b128(rws, sof, 3 * 8 * NB * DM * 8, AUX_SC1);
    }
    float s0 = (u2f(sr[0][0]) + u2f(sr[1][0])) + (u2f(sr[2][0]) + u2f(sr[3][0])), s1 = (u2f(sr[0][2]) + u2f(sr[1][2])) + (u2f(sr[2][2]) + u2f(sr[3][2]));
    s0 += dpp<ROR8>(s0); s1 += dpp<ROR8>(s1);
    s0 = rbf(xrow_sum(s0)); s1 = rbf(xrow_sum(s1));
    const unsigned o = cvtpk(bflo(xfin) + rbf(s0 * gate0), bfhi(xfin) + rbf(s1 * gate1));
    if (lane < 8) *(GAS unsigned*)(out + b * DM + 16 * cb + 2 * lane) = o;
    if (j == 0 && lane == 0) __hip_atomic_store((gu32*)(ws + WS_EPOCH), tag, RLX_AGENT);
  }
}

extern "C" void kernel_launch(void* const* d_in, const int* in_sizes, int n_in, void* d_out, int out_size, void* d_ws, size_t ws_size,
                              hipStream_t stream) {
  (void)in_sizes; (void)n_in; (void)out_size; (void)ws_size;
  fused_decode_kernel<<<NWG, NTHR, 0, stream>>>((const unsigned short*)d_in[0], (const unsigned short*)d_in[1], (const unsigned short*)d_in[2],
                                                (const unsigned short*)d_in[3], (const unsigned short*)d_in[4], (const unsigned short*)d_in[6],
                                                (const unsigned short*)d_in[7], (const unsigned short*)d_in[8], (unsigned short*)d_out,
                                                (unsigned char*)d_ws);
}
```

```cpp
#define CODE_LINES 162
#include <hip/hip_runtime.h>
#include <hip/hip_bf16.h>
#include <stdint.h>

#define GAS __attribute__((address_space(1)))
#define LAS __attribute__((address_space(3)))
#define RLX_AGENT __ATOMIC_RELAXED, __HIP_MEMORY_SCOPE_AGENT
typedef unsigned v4u __attribute__((ext_vector_type(4)));
typedef unsigned v2u __attribute__((ext_vector_type(2)));
typedef float f32x4 __attribute__((ext_vector_type(4)));
typedef float f32x2 __attribute__((ext_vector_type(2)));
typedef short bf16x8 __attribute__((ext_vector_type(8)));
typedef short s16x4 __attribute__((ext_vector_type(4)));
typedef __bf16 bf16x2 __attribute__((ext_vector_type(2)));
typedef unsigned long long u64;
typedef GAS u64 gu64;
typedef GAS unsigned gu32;

namespace {
template <int N> struct IC { static constexpr int value = N; };
constexpr int NB = 8, DM = 512, HQ = 32, HKV = 8, SK = 8192, DH = 128, FF = 1792;
constexpr int NWG = 256, NTHR = 512;
constexpr float SCALE = 0.08838834764831845f;
constexpr float L2E = 1.4426950408889634f;
constexpr unsigned SPIN_CAP = 1u << 18;
constexpr int AUX_NT = 2, AUX_SC1 = 16, RS3 = 0x00020000;

constexpr int WS_EPOCH = 0;
constexpr int WS_TS = 256;
constexpr int WS_QP = 4096;
constexpr int WS_GP = WS_QP + 2 * NB * 4096 * 8;
constexpr int WS_HG = WS_GP + 2 * 2 * NB * FF * 8;
constexpr int WS_MG = WS_HG + NB * 896 * 8;
constexpr int WS_PG = WS_MG + 64 * 16 * 8;
constexpr int WS_SG = WS_PG + NWG * 256 * 8;
constexpr int WS_DG = WS_SG + HQ * NB * DM * 8;
constexpr int WS_END = WS_DG + HQ * NB * DM * 8;

constexpr int XN_STR = 1040;
constexpr int L_XN = 0;
constexpr int L_T0 = 8448;
constexpr int L_T1 = L_T0 + 16384;
constexpr int QS_STR = 272;
constexpr int L_QS = L_T1 + 16384;
constexpr int L_MRG = L_QS + 1152;
constexpr int L_ML = L_MRG + 16384;
constexpr int L_O = L_ML + 256;
constexpr int L_WD = L_O + 8 * QS_STR;
constexpr int L_FLAG = L_WD + 8192;
constexpr int L_WO = L_FLAG + 64;
constexpr int L_END = L_WO + 16384;

__device__ __forceinline__ f32x4 mfma(v4u a, v4u b, f32x4 c) {
  return __builtin_amdgcn_mfma_f32_16x16x32_bf16(__builtin_bit_cast(bf16x8, a), __builtin_bit_cast(bf16x8, b), c, 0, 0, 0);
}
__device__ __forceinline__ unsigned cvtpk(float lo, float hi) {
  f32x2 v = {lo, hi};
  return __builtin_bit_cast(unsigned, __builtin_convertvector(v, bf16x2));
}
__device__ __forceinline__ float bflo(unsigned u) { return __builtin_bit_cast(float, u << 16); }
__device__ __forceinline__ float bfhi(unsigned u) { return __builtin_bit_cast(float, u & 0xffff0000u); }
__device__ __forceinline__ float rbf(float x) { return bflo(cvtpk(x, x)); }
__device__ __forceinline__ float u2f(unsigned u) { return __builtin_bit_cast(float, u); }
template <int CTRL> __device__ __forceinline__ float dpp(float x) {
  return __builtin_bit_cast(float, __builtin_amdgcn_mov_dpp(__builtin_bit_cast(int, x), CTRL, 0xf, 0xf, true));
}
constexpr int ROR1 = 0x121, ROR2 = 0x122, ROR4 = 0x124, ROR8 = 0x128;
__device__ __forceinline__ float xrow_max(float x) {
  auto s = __builtin_amdgcn_permlane16_swap(__float_as_uint(x), __float_as_uint(x), false, false);
  x = fmaxf(__uint_as_float(s[0]), __uint_as_float(s[1]));
  auto t = __builtin_amdgcn_permlane32_swap(__float_as_uint(x), __float_as_uint(x), false, false);
  return fmaxf(__uint_as_float(t[0]), __uint_as_float(t[1]));
}
__device__ __forceinline__ float xrow_sum(float x) {
  auto s = __builtin_amdgcn_permlane16_swap(__float_as_uint(x), __float_as_uint(x), false, false);
  x = __uint_as_float(s[0]) + __uint_as_float(s[1]);
  auto t = __builtin_amdgcn_permlane32_swap(__float_as_uint(x), __float_as_uint(x), false, false);
  return __uint_as_float(t[0]) + __uint_as_float(t[1]);
}
__device__ __forceinline__ float row_sum(float x) {
  x += dpp<ROR8>(x); x += dpp<ROR4>(x); x += dpp<ROR2>(x); x += dpp<ROR1>(x); return x;
}
__device__ __forceinline__ float wave_sum(float x) { return xrow_sum(row_sum(x)); }
__device__ __forceinline__ v4u ldnt(const void* p) { return __builtin_nontemporal_load((const GAS v4u*)p); }
__device__ __forceinline__ void st_gran(gu64* g, unsigned tag, unsigned v) {
  __hip_atomic_store(g, ((u64)tag << 32) | (u64)v, RLX_AGENT);
}
__device__ __forceinline__ u64 ld_gran(gu64* g) { return __hip_atomic_load(g, RLX_AGENT); }
__device__ __forceinline__ void publish_tile(__amdgpu_buffer_rsrc_t rs, int off, f32x4 a, unsigned tag, int c) {
  const bool hi = c >= 8;
  const v4u v = {__float_as_uint(hi ? a[2] : a[0]), tag, __float_as_uint(hi ? a[3] : a[1]), tag};
  __builtin_amdgcn_raw_buffer_store_b128(v, rs, off + (hi ? 16 : 0), 0, AUX_SC1);
}
__device__ __forceinline__ v4u tile_frag(LAS unsigned char* wa, int ks) {
  const v2u lo = __builtin_bit_cast(v2u, __builtin_amdgcn_ds_read_tr16_b64_v4i16((LAS s16x4*)(wa + ks * 4096)));
  const v2u hi = __builtin_bit_cast(v2u, __builtin_amdgcn_ds_read_tr16_b64_v4i16((LAS s16x4*)(wa + ks * 4096 + 512)));
  return v4u{lo.x, lo.y, hi.x, hi.y};
}
__device__ __forceinline__ void lds_arrive(LAS unsigned* p, int lane) {
  __builtin_amdgcn_fence(__ATOMIC_RELEASE, "workgroup");
  if (lane == 0) __hip_atomic_fetch_add(p, 1u, __ATOMIC_RELAXED, __HIP_MEMORY_SCOPE_WORKGROUP);
}
__device__ __forceinline__ void lds_wait(LAS unsigned* p, unsigned n) {
  while (__hip_atomic_load(p, __ATOMIC_RELAXED, __HIP_MEMORY_SCOPE_WORKGROUP) < n) __builtin_amdgcn_s_sleep(1);
  __builtin_amdgcn_fence(__ATOMIC_ACQUIRE, "workgroup");
}
__device__ __forceinline__ v4u layernorm_row(v4u xv) {
  float xf[8];
#pragma unroll
  for (int k = 0; k < 4; ++k) { xf[2 * k] = bflo(xv[k]); xf[2 * k + 1] = bfhi(xv[k]); }
  float s = 0.f;
#pragma unroll
  for (int k = 0; k < 8; ++k) s += xf[k];
  const float mean = wave_sum(s) * (1.0f / DM);
  float q2 = 0.f;
#pragma unroll
  for (int k = 0; k < 8; ++k) { const float d = xf[k] - mean; q2 = fmaf(d, d, q2); }
  const float var = wave_sum(q2) * (1.0f / DM);
  const float mb = rbf(mean), inv = __builtin_amdgcn_rcpf(rbf(__builtin_amdgcn_sqrtf(rbf(rbf(var) + 1e-5f))));
  v4u o;
#pragma unroll
  for (int k = 0; k < 4; ++k) { const unsigned dd = cvtpk(xf[2 * k] - mb, xf[2 * k + 1] - mb); o[k] = cvtpk(bflo(dd) * inv, bfhi(dd) * inv); }
  return o;
}
}

#define PACE 3850
#define SLOT_GAP 4
#define SLOT_GAP2 1
#define Q_AT 480
#define WO_D0 60
#define WO_STEP 10
#define S2_DELAY 155
#define S4_DELAY 310
#define LATE2 20
#define LATE4 50
#define FINAL_WAVE 5
#define NOW() ((unsigned)__builtin_amdgcn_s_memrealtime())
#define SPIN_PAUSE() do { __builtin_amdgcn_s_sleep(1); asm volatile("" ::: "memory"); } while (0)

__global__ void __launch_bounds__(NTHR, 2)
fused_decode_kernel(const unsigned short* __restrict__ x, const unsigned short* __restrict__ Kc, const unsigned short* __restrict__ Vc,
                    const unsigned short* __restrict__ wq, const unsigned short* __restrict__ wo, const unsigned short* __restrict__ wg,
                    const unsigned short* __restrict__ wu, const unsigned short* __restrict__ wd, unsigned short* __restrict__ out,
                    unsigned char* ws) {
  __shared__ __attribute__((aligned(16))) unsigned char lds_raw[L_END];
  LAS unsigned char* L = (LAS unsigned char*)lds_raw;
  const int tid = threadIdx.x, lane = tid & 63, w = __builtin_amdgcn_readfirstlane(tid >> 6);
  const int g = lane >> 4, c = lane & 15;
  const int j = blockIdx.x, xc = j & 7, idx = j >> 3;
  const int b = idx >> 2, sp = idx & 3;
  const int pair = xc * HKV + (idx >> 2);
  const int h2 = 4 * xc + (idx & 3), cq = idx >> 2;
  const int cb = 4 * xc + (idx & 3);
  const bool isq = (idx & 1) == 0;
  const int fu = xc * 14 + (idx >> 1), fmat = fu & 1;
  const bool has_tile = isq || (idx >> 1) < 14;
  const int tkc = isq ? (idx >> 1) & 1 : (fu >> 1) & 1;
  const int tcol = isq ? 512 * xc + 64 * (idx >> 2) : 64 * (fu >> 2);
  const int tld = isq ? HQ * DH : FF;
  const int q4 = c >> 2, p4 = lane & 3, mt = w & 3;
  const __amdgpu_buffer_rsrc_t rws = __builtin_amdgcn_make_buffer_rsrc((void*)ws, 0, WS_END, RS3);

  const unsigned tstart = NOW();
  unsigned long long pc0 = __builtin_amdgcn_s_getpc();
  asm volatile("" : "+s"(pc0));
  unsigned epoch = *(const __attribute__((address_space(4))) unsigned*)(ws + WS_EPOCH);
  LAS unsigned* flags = (LAS unsigned*)(L + L_FLAG);
  if (tid < 16) flags[tid] = 0u;
  __syncthreads();

  if (w < 4) {
    const unsigned tag = epoch + 1u;
    const v4u xv0 = *(const GAS v4u*)(x + (2 * w) * DM + lane * 8), xv1 = *(const GAS v4u*)(x + (2 * w + 1) * DM + lane * 8);
    const __amdgpu_buffer_rsrc_t rt = __builtin_amdgcn_make_buffer_rsrc((void*)(isq ? wq : (fmat ? wu : wg)), 0, has_tile ? DM * tld * 2 : 0, RS3);
    const int toff = ((256 * tkc + 64 * w + (lane >> 3)) * tld + tcol + (lane & 7) * 8) * 2;
    v4u ta[8];
#pragma unroll
    for (int u = 0; u < 8; ++u) ta[u] = __builtin_amdgcn_raw_buffer_load_b128(rt, toff, u * 8 * tld * 2, AUX_NT);
    unsigned ptv;
    {
      const size_t kvb = ((size_t)pair * SK + sp * 256) * (DH * 2);
      const int pl = (w == 3 && lane < 19) ? lane : 8;
      const char* pa = pl < 8 ? (const char*)Vc + kvb + pl * (1024 * 256) : pl < 16 ? (const char*)Kc + kvb + (pl - 8) * (1024 * 256)
                     : pl == 16 ? (const char*)(wo + (size_t)(128 * h2) * DM + 64 * cq) : pl == 17 ? (const char*)(wd + (size_t)(56 * h2) * DM + 64 * cq) : (const char*)(out + b * DM + 16 * cb);
      ptv = __builtin_nontemporal_load((const GAS unsigned*)pa);
    }
    __builtin_amdgcn_sched_barrier(0);
    const v4u o0 = layernorm_row(xv0), o1 = layernorm_row(xv1);
    *(LAS v4u*)(L + L_XN + (2 * w) * XN_STR + lane * 16) = o0;
    *(LAS v4u*)(L + L_XN + (2 * w + 1) * XN_STR + lane * 16) = o1;
#pragma unroll
    for (int u = 0; u < 8; ++u) *(LAS v4u*)(L + L_T0 + w * 8192 + u * 1024 + lane * 16) = ta[u];
    lds_arrive(flags + 1, lane);
    lds_wait(flags + 1, 4u);
    {
      LAS unsigned char* wa = L + L_T0 + (8 * g + q4) * 128 + mt * 32 + 8 * p4;
      LAS unsigned char* xb = L + L_XN + (c & 7) * XN_STR + tkc * 512 + g * 16;
      f32x4 a0 = {0.f, 0.f, 0.f, 0.f}, a1 = {0.f, 0.f, 0.f, 0.f};
#pragma unroll
      for (int ks = 0; ks < 8; ks += 2) {
        a0 = mfma(tile_frag(wa, ks), *(LAS v4u*)(xb + ks * 64), a0);
        a1 = mfma(tile_frag(wa, ks + 1), *(LAS v4u*)(xb + ks * 64 + 64), a1);
      }
      a0 += a1;
      const int dst = isq ? WS_QP + ((tkc * NB + (c & 7)) * 4096) * 8 : WS_GP + (((fmat * 2 + tkc) * NB + (c & 7)) * FF) * 8;
      if (has_tile) publish_tile(rws, dst + (tcol + 16 * mt + 4 * g) * 8, a0, tag, c);
    }
    asm volatile("" :: "v"(ptv));
    if (w == 0) {
      int qo = WS_QP + (pair * 512 + 2 * lane) * 8;
      v4u qr[2][4];
      u64 tsg;
      while ((int)(NOW() - tstart) < Q_AT) __builtin_amdgcn_s_sleep(1);
      for (unsigned spins = 0;; ++spins) {
        asm volatile("" : "+v"(qo));
#pragma unroll
        for (int kc = 0; kc < 2; ++kc)
#pragma unroll
          for (int hh = 0; hh < 4; ++hh) qr[kc][hh] = __builtin_amdgcn_raw_buffer_load_b128(rws, qo, (kc * NB * 4096 + hh * 128) * 8, AUX_SC1);
        tsg = ld_gran((gu64*)(ws + WS_TS) + (lane & 7));
        bool ok = true;
#pragma unroll
        for (int kc = 0; kc < 2; ++kc)
#pragma unroll
          for (int hh = 0; hh < 4; ++hh) ok &= (qr[kc][hh][1] == tag) & (qr[kc][hh][3] == tag);
        if (__all(ok) || spins > SPIN_CAP) break;
        SPIN_PAUSE();
      }
#pragma unroll
      for (int hh = 0; hh < 4; ++hh)
        *(LAS unsigned*)(L + L_QS + hh * QS_STR + lane * 4) = cvtpk(u2f(qr[0][hh][0]) + u2f(qr[1][hh][0]), u2f(qr[0][hh][2]) + u2f(qr[1][hh][2]));
      {
        int dt = ((unsigned)(tsg >> 32) == tag) ? (int)((unsigned)tsg - tstart) : 0;
        dt = min(max(dt, -400), 400);
        dt = min(dt, __builtin_amdgcn_mov_dpp(dt, 0xB1, 0xf, 0xf, true)); dt = min(dt, __builtin_amdgcn_mov_dpp(dt, 0x4E, 0xf, 0xf, true)); dt = min(dt, __builtin_amdgcn_mov_dpp(dt, 0x141, 0xf, 0xf, true));
        if (lane == 0) flags[4] = tstart + (unsigned)dt;
      }
      lds_arrive(flags, lane);
    }
  }
  __builtin_amdgcn_sched_barrier(0);

  const size_t kvbase = ((size_t)pair * SK + sp * 256) * (DH * 2);
  const int tn0_ = (w >= 4) ? (w - 4) : (16 + 3 - w), tn1_ = 32 + ((w >= 4) ? (w - 4) : (7 - w));
#define TILE_N(t) ((t) < 4 ? tn0_ + 4 * (t) : tn1_ + 8 * ((t) - 4))
#define TILE_OFF(t) ((TILE_N(t) >> 3) * (1024 * 256) + (TILE_N(t) & 7) * 8192)
  const __amdgpu_buffer_rsrc_t rk = __builtin_amdgcn_make_buffer_rsrc((void*)((const char*)Kc + kvbase), 0, 0x7fffffff, RS3);
  const __amdgpu_buffer_rsrc_t rv = __builtin_amdgcn_make_buffer_rsrc((void*)((const char*)Vc + kvbase), 0, 0x7fffffff, RS3);
  const int koff = (c >> 1) * 256 + (c & 1) * 64 + g * 16;
  const int voff = g * 512 + c * 16;
  v4u buf[4][8];
#define ISSUE(gi) { _Pragma("unroll") for (int u = 0; u < 8; ++u) buf[(gi) & 3][u] = (gi) < 8 \
    ? __builtin_amdgcn_raw_buffer_load_b128(rk, koff + ((u >> 1) & 1) * 2048 + (u & 1) * 128, TILE_OFF((gi) & 7) + (u >> 2) * 4096, AUX_NT) \
    : __builtin_amdgcn_raw_buffer_load_b128(rv, voff + ((u >> 1) & 1) * 2048 + (u & 1) * 256, TILE_OFF((gi) & 7) + (u >> 2) * 4096, AUX_NT); __builtin_amdgcn_sched_barrier(0); }
  unsigned tbase = tstart;
  const unsigned phase = (unsigned)((((xc + (idx >> 2)) & 7) * PACE) >> 10);
#define SLOT_OF(gi) (TILE_N((gi) & 7) + ((gi) >= 8 ? 64 : 0) + ((gi) >= 4 || w < 4 ? SLOT_GAP : 0) + ((gi) >= 6 ? SLOT_GAP2 : 0))
#define PACE_WAIT(gi) { const unsigned due_ = tbase + phase + (unsigned)((SLOT_OF(gi) * PACE) >> 7); while ((int)(NOW() - due_) < 0) __builtin_amdgcn_s_sleep(1); __builtin_amdgcn_sched_barrier(0); }
  PACE_WAIT(0) ISSUE(0)
  asm volatile("" : "+s"(epoch));
  const unsigned tag = epoch + 1u;
  if (w == 4 && idx == 0 && lane == 0) st_gran((gu64*)(ws + WS_TS) + xc, tag, tstart);
  __builtin_amdgcn_sched_barrier(0);
  PACE_WAIT(1) ISSUE(1) PACE_WAIT(2) ISSUE(2) PACE_WAIT(3) ISSUE(3)
  if (w == 7 && idx < 2) {
    const char* cp = (const char*)(pc0 & ~127ull);
    unsigned cv = 0;
#pragma unroll
    for (int k = 0; k < 3; ++k) cv ^= *(const GAS unsigned*)(cp + min(k * 64 + lane, CODE_LINES) * 128);
    asm volatile("" :: "v"(cv));
  }
  __builtin_amdgcn_sched_barrier(0);
  lds_wait(flags, 1u);
  tbase = __builtin_amdgcn_readfirstlane(flags[4]);

  v4u qf[2];
#pragma unroll
  for (int hf = 0; hf < 2; ++hf) qf[hf] = *(LAS v4u*)(L + L_QS + (c & 3) * QS_STR + hf * 128 + ((c >> 2) & 1) * 64 + g * 16);
  const bool up = (c >> 2) & 1;
  f32x4 acc[8];
#pragma unroll
  for (int t = 0; t < 8; ++t) acc[t] = f32x4{0.f, 0.f, 0.f, 0.f};
  float m = -INFINITY, fw = 0.f;
  v4u sp_[8];

  auto qk = [&](const v4u (&kt)[8], v4u& sc) __attribute__((always_inline)) {
#pragma unroll
    for (int G = 0; G < 4; ++G) {
      f32x4 dg = mfma(kt[2 * G], qf[0], f32x4{0.f, 0.f, 0.f, 0.f});
      dg = mfma(kt[2 * G + 1], qf[1], dg);
      const float e0 = up ? dg[1] : dg[0], e1 = up ? dg[3] : dg[2];
      const float s0 = e0 + dpp<ROR4>(e0), s1 = e1 + dpp<ROR4>(e1);
      const unsigned a0 = cvtpk(s0, s1);
      sc[G] = cvtpk(bflo(a0) * SCALE, bfhi(a0) * SCALE);
      m = fmaxf(m, fmaxf(bflo(sc[G]), bfhi(sc[G])));
    }
    asm volatile("" : "+v"(sc), "+v"(m));
    __builtin_amdgcn_sched_barrier(0);
  };
  auto pv = [&](const v4u (&vt)[8], const v4u& pf) __attribute__((always_inline)) {
#pragma unroll
    for (int t = 0; t < 8; ++t) {
      const unsigned sel = (t & 1) ? 0x07060302u : 0x05040100u;
      v4u a;
#pragma unroll
      for (int mm = 0; mm < 4; ++mm) a[mm] = __builtin_amdgcn_perm(vt[2 * mm + 1][t >> 1], vt[2 * mm][t >> 1], sel);
      acc[t] = mfma(a, pf, acc[t]);
    }
#pragma unroll
    for (int t = 0; t < 8; ++t) asm volatile("" : "+v"(acc[t]));
    __builtin_amdgcn_sched_barrier(0);
  };
#define CONSUME(gi) { if ((gi) < 8) qk(buf[(gi) & 3], sp_[(gi) & 7]); else pv(buf[(gi) & 3], sp_[(gi) & 7]); }

  v4u wt, af[4];
  float gate0 = 0.f, gate1 = 0.f;
  unsigned xfin = 0u;
  const int fo = ((lane >> 3) * NB + b) * DM + 16 * cb + 2 * (lane & 7);
  const int ord = (w >= 4) ? (w - 4) : (7 - w);

  CONSUME(0)
  auto step = [&](auto GI) __attribute__((always_inline)) {
    constexpr int gi = decltype(GI)::value;
    PACE_WAIT(gi) ISSUE(gi)
    if (gi < 15) CONSUME(gi - 3)
    if (gi == 14) CONSUME(12)
    if (gi >= 14) CONSUME(gi - 1)
    if (gi == 10) {
      m = xrow_max(m);
      const float nm = -m * L2E;
      float l0 = 0.f, l1 = 0.f;
#pragma unroll
      for (int t = 0; t < 8; ++t)
#pragma unroll
        for (int G = 0; G < 4; ++G) {
          const float p0 = __builtin_amdgcn_exp2f(fmaf(bflo(sp_[t][G]), L2E, nm)), p1 = __builtin_amdgcn_exp2f(fmaf(bfhi(sp_[t][G]), L2E, nm));
          l0 += p0; l1 += p1;
          sp_[t][G] = cvtpk(p0, p1);
        }
      const float l = xrow_sum(l0 + l1);
      if (lane < 4) *(LAS f32x2*)(L + L_ML + (w * 4 + lane) * 8) = f32x2{m, l};
      lds_arrive(flags + 2, lane);
    }
    if (gi == 5 && w == 1) {
      int go = WS_GP + (b * FF + 56 * h2 + 2 * (lane < 28 ? lane : 27)) * 8;
      v4u gr[2][2];
      for (unsigned spins = 0;; ++spins) {
        asm volatile("" : "+v"(go));
#pragma unroll
        for (int mm = 0; mm < 2; ++mm)
#pragma unroll
          for (int kc = 0; kc < 2; ++kc) gr[mm][kc] = __builtin_amdgcn_raw_buffer_load_b128(rws, go, ((mm * 2 + kc) * NB * FF) * 8, AUX_SC1);
        bool ok = true;
#pragma unroll
        for (int mm = 0; mm < 2; ++mm)
#pragma unroll
          for (int kc = 0; kc < 2; ++kc) ok &= (gr[mm][kc][1] == tag) & (gr[mm][kc][3] == tag);
        if (__all(ok) || spins > SPIN_CAP) break;
        SPIN_PAUSE();
      }
      const float g0 = rbf(u2f(gr[0][0][0]) + u2f(gr[0][1][0])), g1 = rbf(u2f(gr[0][0][2]) + u2f(gr[0][1][2]));
      const float u0 = rbf(u2f(gr[1][0][0]) + u2f(gr[1][1][0])), u1 = rbf(u2f(gr[1][0][2]) + u2f(gr[1][1][2]));
      const float h0 = (g0 > 0.f ? g0 : 0.f * g0) * u0, h1 = (g1 > 0.f ? g1 : 0.f * g1) * u1;
      if (lane < 28) st_gran((gu64*)(ws + WS_HG) + b * 896 + 28 * h2 + lane, tag, cvtpk(h0, h1));
    }
    if (gi == 5)
      wt = ldnt(wd + (size_t)(56 * h2 + 8 * (w < 7 ? w : 6) + (lane >> 3)) * DM + 64 * cq + (lane & 7) * 8);
    if (gi == 7) {
      *(LAS v4u*)(L + L_WD + w * 1024 + lane * 16) = wt;
      lds_arrive(flags + 3, lane);
    }
    if (gi == 11 && w == 0) {
      lds_wait(flags + 2, 8u);
      f32x2 ml[8];
      float M = -INFINITY, Ls = 0.f;
#pragma unroll
      for (int k = 0; k < 8; ++k) { ml[k] = *(LAS f32x2*)(L + L_ML + (k * 4 + (lane & 3)) * 8); M = fmaxf(M, ml[k].x); }
#pragma unroll
      for (int k = 0; k < 8; ++k) Ls = fmaf(__builtin_amdgcn_exp2f((ml[k].x - M) * L2E), ml[k].y, Ls);
      if (lane < 4) st_gran((gu64*)(ws + WS_MG) + (pair * 4 + sp) * 4 + lane, tag, __float_as_uint(fmaf(M, L2E, __builtin_amdgcn_logf(Ls))));
    }
    if (gi == 11 && w >= 4) {
      int ho = WS_HG + ((c & 7) * 896 + 28 * h2 + 4 * g) * 8, ho1 = ho + (g < 3 ? 128 : 0);
      v4u hgr[4];
      for (unsigned spins = 0;; ++spins) {
        asm volatile("" : "+v"(ho), "+v"(ho1));
        hgr[0] = __builtin_amdgcn_raw_buffer_load_b128(rws, ho, 0, AUX_SC1); hgr[1] = __builtin_amdgcn_raw_buffer_load_b128(rws, ho, 16, AUX_SC1);
        hgr[2] = __builtin_amdgcn_raw_buffer_load_b128(rws, ho1, 0, AUX_SC1); hgr[3] = __builtin_amdgcn_raw_buffer_load_b128(rws, ho1, 16, AUX_SC1);
        bool ok = true;
#pragma unroll
        for (int k = 0; k < 4; ++k) ok &= (hgr[k][1] == tag) & (hgr[k][3] == tag);
        if (__all(ok) || spins > SPIN_CAP) break;
        SPIN_PAUSE();
      }
      lds_wait(flags + 3, 8u);
      LAS unsigned char* wa = L + L_WD + (8 * g + q4) * 128 + mt * 32 + 8 * p4;
      const v4u b0 = {hgr[0][0], hgr[0][2], hgr[1][0], hgr[1][2]};
      v4u b1 = {hgr[2][0], hgr[2][2], hgr[3][0], hgr[3][2]};
      if (g == 3) b1 = v4u{0u, 0u, 0u, 0u};
      f32x4 dd = mfma(tile_frag(wa, 0), b0, f32x4{0.f, 0.f, 0.f, 0.f});
      dd = mfma(tile_frag(wa, 1), b1, dd);
      publish_tile(rws, WS_DG + ((h2 * NB + (c & 7)) * DM + 64 * cq + 16 * mt + 4 * g) * 8, dd, tag, c);
    }
    if (gi == 13) {
      gu64* mg = (gu64*)(ws + WS_MG) + pair * 16 + c;
      u64 lg;
      for (unsigned spins = 0;; ++spins) {
        lg = ld_gran(mg);
        if (__all((unsigned)(lg >> 32) == tag) || spins > SPIN_CAP) break;
        SPIN_PAUSE();
      }
      const float ls = u2f((unsigned)lg);
      float mx = fmaxf(ls, dpp<ROR4>(ls)); mx = fmaxf(mx, dpp<ROR8>(mx));
      const float e = __builtin_amdgcn_exp2f(ls - mx), e2 = e + dpp<ROR8>(e), se = e2 + dpp<ROR4>(e2);
      fw = __builtin_amdgcn_exp2f(fmaf(m, L2E, -mx)) * __builtin_amdgcn_rcpf(se);
      asm volatile("" : "+v"(fw));
    }
    if (gi == 14 && w == FINAL_WAVE) {
      int dof = WS_DG + fo * 8;
      v4u dr[4];
      xfin = *(const GAS unsigned*)(x + b * DM + 16 * cb + 2 * (lane & 7));
      for (unsigned spins = 0;; ++spins) {
        asm volatile("" : "+v"(dof));
#pragma unroll
        for (int k = 0; k < 4; ++k) dr[k] = __builtin_amdgcn_raw_buffer_load_b128(rws, dof, k * 8 * NB * DM * 8, AUX_SC1);
        bool ok = true;
#pragma unroll
        for (int k = 0; k < 4; ++k) ok &= (dr[k][1] == tag) & (dr[k][3] == tag);
        if (__all(ok) || spins > SPIN_CAP) break;
        SPIN_PAUSE();
      }
      float d0 = (u2f(dr[0][0]) + u2f(dr[1][0])) + (u2f(dr[2][0]) + u2f(dr[3][0])), d1 = (u2f(dr[0][2]) + u2f(dr[1][2])) + (u2f(dr[2][2]) + u2f(dr[3][2]));
      d0 += dpp<ROR8>(d0); d1 += dpp<ROR8>(d1);
      d0 = rbf(xrow_sum(d0)); d1 = rbf(xrow_sum(d1));
      gate0 = rbf(d0 / (1.0f + __expf(-d0))); gate1 = rbf(d1 / (1.0f + __expf(-d1)));
    }
    __builtin_amdgcn_sched_barrier(0);
  };
  step(IC<4>{}); step(IC<5>{}); step(IC<6>{}); step(IC<7>{}); step(IC<8>{}); step(IC<9>{});
  step(IC<10>{}); step(IC<11>{}); step(IC<12>{}); step(IC<13>{}); step(IC<14>{}); step(IC<15>{});
  const int u = c >> 2;
  LAS unsigned char* rec = L + L_MRG + (c & 3) * 512 + g * 128 + u * 16;
#define PICKA(k) ((u & 1) ? ((u & 2) ? acc[4 + (k)][1] : acc[4 + (k)][0]) : ((u & 2) ? acc[k][1] : acc[k][0]))
#define PICKB(k) ((u & 1) ? ((u & 2) ? acc[4 + (k)][3] : acc[4 + (k)][2]) : ((u & 2) ? acc[k][3] : acc[k][2]))
  f32x4 xA = {0.f, 0.f, 0.f, 0.f}, xB = {0.f, 0.f, 0.f, 0.f};
  if (ord == 7) {
#pragma unroll
    for (int k = 0; k < 7; ++k) {
      lds_wait(flags + 8 + k, 1u);
      xA += *(LAS f32x4*)(rec + k * 2048); xB += *(LAS f32x4*)(rec + k * 2048 + 64);
    }
    asm volatile("" : "+v"(xA), "+v"(xB));
  }
  __builtin_amdgcn_sched_barrier(0);
  CONSUME(15)
  const f32x4 pA = f32x4{PICKA(0), PICKA(1), PICKA(2), PICKA(3)} * fw + xA, pB = f32x4{PICKB(0), PICKB(1), PICKB(2), PICKB(3)} * fw + xB;
  if (ord == 7) {
    const int po = WS_PG + ((pair * 4 + sp) * 256 + (c & 3) * 64) * 8 + g * 128 + u * 16;
    __builtin_amdgcn_raw_buffer_store_b128(v4u{cvtpk(pA[0], pA[1]), tag, cvtpk(pA[2], pA[3]), tag}, rws, po, 0, AUX_SC1);
    __builtin_amdgcn_raw_buffer_store_b128(v4u{cvtpk(pB[0], pB[1]), tag, cvtpk(pB[2], pB[3]), tag}, rws, po + 64, 0, AUX_SC1);
    lds_arrive(flags + 15, lane);
  } else {
    *(LAS f32x4*)(rec + ord * 2048) = pA;
    *(LAS f32x4*)(rec + ord * 2048 + 64) = pB;
    lds_arrive(flags + 8 + ord, lane);
  }
  const unsigned tpub0 = tbase + (unsigned)(((127 + SLOT_GAP + SLOT_GAP2) * PACE) >> 7);
  v4u wl[4];
  {
    if (w >= 4) while ((int)(NOW() - tpub0) < WO_D0 + mt * WO_STEP) __builtin_amdgcn_s_sleep(1);
    const __amdgpu_buffer_rsrc_t rwo = __builtin_amdgcn_make_buffer_rsrc((void*)wo, 0, w >= 4 ? HQ * DH * DM * 2 : 0, RS3);
    const int wof = ((128 * h2 + 32 * mt + (lane >> 3)) * DM + 64 * cq + (lane & 7) * 8) * 2;
#pragma unroll
    for (int k = 0; k < 4; ++k) wl[k] = __builtin_amdgcn_raw_buffer_load_b128(rwo, wof, k * 8 * DM * 2, AUX_NT);
    __builtin_amdgcn_sched_barrier(0);
  }
  {
    const int hf = lane >> 5;
    int po = WS_PG + (((w * HKV + xc) * 4 + hf) * 256 + (h2 & 3) * 64 + 2 * (lane & 31)) * 8;
    v4u gr[2];
    lds_wait(flags + 15, 1u);
    {
      const int t2 = max((int)(S2_DELAY), (int)(NOW() - tpub0) + LATE2);
      while ((int)(NOW() - tpub0) < t2) {}
    }
    gr[0] = __builtin_amdgcn_raw_buffer_load_b128(rws, po, 0, AUX_SC1);
    gr[1] = __builtin_amdgcn_raw_buffer_load_b128(rws, po, 2 * 256 * 8, AUX_SC1);
    if (w >= 4) {
      __builtin_amdgcn_sched_barrier(0);
#pragma unroll
      for (int k = 0; k < 4; ++k) *(LAS v4u*)(L + L_WO + mt * 4096 + k * 1024 + lane * 16) = wl[k];
      lds_arrive(flags + 5, lane);
      __builtin_amdgcn_sched_barrier(0);
    }
    for (unsigned spins = 0;; ++spins) {
      const bool k0 = (gr[0][1] == tag) & (gr[0][3] == tag), k1 = (gr[1][1] == tag) & (gr[1][3] == tag);
      if (__all(k0 & k1) || spins > SPIN_CAP) break;
      asm volatile("" : "+v"(po) :: "memory");
      if (!k0) gr[0] = __builtin_amdgcn_raw_buffer_load_b128(rws, po, 0, AUX_SC1);
      if (!k1) gr[1] = __builtin_amdgcn_raw_buffer_load_b128(rws, po, 2 * 256 * 8, AUX_SC1);
    }
    const float o0 = bflo(gr[0][0]) + bflo(gr[1][0]), o1 = bfhi(gr[0][0]) + bfhi(gr[1][0]), o2 = bflo(gr[0][2]) + bflo(gr[1][2]), o3 = bfhi(gr[0][2]) + bfhi(gr[1][2]);
    auto s01 = __builtin_amdgcn_permlane32_swap(__float_as_uint(o0), __float_as_uint(o2), false, false);
    auto s23 = __builtin_amdgcn_permlane32_swap(__float_as_uint(o1), __float_as_uint(o3), false, false);
    *(LAS unsigned*)(L + L_O + w * QS_STR + (lane & 31) * 8 + hf * 4) = cvtpk(u2f(s01[0]) + u2f(s01[1]), u2f(s23[0]) + u2f(s23[1]));
  }
  if (w < 4) {
    lds_wait(flags + 5, 4u);
    LAS unsigned char* wa = L + L_WO + (8 * g + q4) * 128 + mt * 32 + 8 * p4;
#pragma unroll
    for (int ks = 0; ks < 4; ++ks) { af[ks] = tile_frag(wa, ks); asm volatile("" : "+v"(af[ks])); }
  }
  __syncthreads();
  if (w < 4) {
    LAS unsigned char* ob = L + L_O + (c & 7) * QS_STR + g * 16;
    f32x4 s0 = mfma(af[0], *(LAS v4u*)(ob), f32x4{0.f, 0.f, 0.f, 0.f});
    f32x4 s1 = mfma(af[1], *(LAS v4u*)(ob + 64), f32x4{0.f, 0.f, 0.f, 0.f});
    s0 = mfma(af[2], *(LAS v4u*)(ob + 128), s0);
    s1 = mfma(af[3], *(LAS v4u*)(ob + 192), s1);
    s0 += s1;
    publish_tile(rws, WS_SG + ((h2 * NB + (c & 7)) * DM + 64 * cq + 16 * mt + 4 * g) * 8, s0, tag, c);
  }
  if (w == FINAL_WAVE) {
    int sof = WS_SG + fo * 8;
    v4u sr[4];
    {
      const int t4 = max((int)(S4_DELAY), (int)(NOW() - tpub0) + LATE4);
      while ((int)(NOW() - tpub0) < t4) {}
    }
#pragma unroll
    for (int k = 0; k < 4; ++k) sr[k] = __builtin_amdgcn_raw_buffer_load_b128(rws, sof, k * 8 * NB * DM * 8, AUX_SC1);
    for (unsigned spins = 0;; ++spins) {
      const bool k0 = (sr[0][1] == tag) & (sr[0][3] == tag), k1 = (sr[1][1] == tag) & (sr[1][3] == tag),
                 k2 = (sr[2][1] == tag) & (sr[2][3] == tag), k3 = (sr[3][1] == tag) & (sr[3][3] == tag);
      if (__all(k0 & k1 & k2 & k3) || spins > SPIN_CAP) break;
      asm volatile("" : "+v"(sof) :: "memory");
      if (!k0) sr[0] = __builtin_amdgcn_raw_buffer_load_b128(rws, sof, 0, AUX_SC1);
      if (!k1) sr[1] = __builtin_amdgcn_raw_buffer_load_b128(rws, sof, 8 * NB * DM * 8, AUX_SC1);
      if (!k2) sr[2] = __builtin_amdgcn_raw_buffer_load_b128(rws, sof, 2 * 8 * NB * DM * 8, AUX_SC1);
      if (!k3) sr[3] = __builtin_amdgcn_raw_buffer_load_b128(rws, sof, 3 * 8 * NB * DM * 8, AUX_SC1);
    }
    float s0 = (u2f(sr[0][0]) + u2f(sr[1][0])) + (u2f(sr[2][0]) + u2f(sr[3][0])), s1 = (u2f(sr[0][2]) + u2f(sr[1][2])) + (u2f(sr[2][2]) + u2f(sr[3][2]));
    s0 += dpp<ROR8>(s0); s1 += dpp<ROR8>(s1);
    s0 = rbf(xrow_sum(s0)); s1 = rbf(xrow_sum(s1));
    const unsigned o = cvtpk(bflo(xfin) + rbf(s0 * gate0), bfhi(xfin) + rbf(s1 * gate1));
    if (lane < 8) *(GAS unsigned*)(out + b * DM + 16 * cb + 2 * lane) = o;
    if (j == 0 && lane == 0) __hip_atomic_store((gu32*)(ws + WS_EPOCH), tag, RLX_AGENT);
  }
}

extern "C" void kernel_launch(void* const* d_in, const int* in_sizes, int n_in, void* d_out, int out_size, void* d_ws, size_t ws_size,
                              hipStream_t stream) {
  (void)in_sizes; (void)n_in; (void)out_size; (void)ws_size;
  fused_decode_kernel<<<NWG, NTHR, 0, stream>>>((const unsigned short*)d_in[0], (const unsigned short*)d_in[1], (const unsigned short*)d_in[2],
                                                (const unsigned short*)d_in[3], (const unsigned short*)d_in[4], (const unsigned short*)d_in[6],
                                                (const unsigned short*)d_in[7], (const unsigned short*)d_in[8], (unsigned short*)d_out,
                                                (unsigned char*)d_ws);
}
```

```cpp
#include <hip/hip_runtime.h>
#include <hip/hip_bf16.h>
#include <stdint.h>

#define GAS __attribute__((address_space(1)))
#define LAS __attribute__((address_space(3)))
#define RLX_AGENT __ATOMIC_RELAXED, __HIP_MEMORY_SCOPE_AGENT
typedef unsigned v4u __attribute__((ext_vector_type(4)));
typedef unsigned v2u __attribute__((ext_vector_type(2)));
typedef float f32x4 __attribute__((ext_vector_type(4)));
typedef float f32x2 __attribute__((ext_vector_type(2)));
typedef short bf16x8 __attribute__((ext_vector_type(8)));
typedef short s16x4 __attribute__((ext_vector_type(4)));
typedef __bf16 bf16x2 __attribute__((ext_vector_type(2)));
typedef unsigned long long u64;
typedef GAS u64 gu64;
typedef GAS unsigned gu32;

namespace {
template <int N> struct IC { static constexpr int value = N; };
constexpr int NB = 8, DM = 512, HQ = 32, HKV = 8, SK = 8192, DH = 128, FF = 1792;
constexpr int NWG = 256, NTHR = 512;
constexpr float SCALE = 0.08838834764831845f;
constexpr float L2E = 1.4426950408889634f;
constexpr unsigned SPIN_CAP = 1u << 18;
constexpr int AUX_NT = 2, AUX_SC1 = 16, RS3 = 0x00020000;

constexpr int WS_EPOCH = 0;
constexpr int WS_TS = 256;
constexpr int WS_QP = 4096;
constexpr int WS_GP = WS_QP + 2 * NB * 4096 * 8;
constexpr int WS_HG = WS_GP + 2 * 2 * NB * FF * 8;
constexpr int WS_MG = WS_HG + NB * 896 * 8;
constexpr int WS_PG = WS_MG + 64 * 16 * 8;
constexpr int WS_SG = WS_PG + NWG * 256 * 8;
constexpr int WS_DG = WS_SG + HQ * NB * DM * 8;
constexpr int WS_END = WS_DG + HQ * NB * DM * 8;

constexpr int XN_STR = 1040;
constexpr int L_XN = 0;
constexpr int L_T0 = 8448;
constexpr int L_T1 = L_T0 + 16384;
constexpr int QS_STR = 272;
constexpr int L_QS = L_T1 + 16384;
constexpr int L_MRG = L_QS + 1152;
constexpr int L_ML = L_MRG + 16384;
constexpr int L_O = L_ML + 256;
constexpr int L_WD = L_O + 8 * QS_STR;
constexpr int L_FLAG = L_WD + 8192;
constexpr int L_WO = L_FLAG + 64;
constexpr int L_END = L_WO + 16384;

__device__ __forceinline__ f32x4 mfma(v4u a, v4u b, f32x4 c) {
  return __builtin_amdgcn_mfma_f32_16x16x32_bf16(__builtin_bit_cast(bf16x8, a), __builtin_bit_cast(bf16x8, b), c, 0, 0, 0);
}
__device__ __forceinline__ unsigned cvtpk(float lo, float hi) {
  f32x2 v = {lo, hi};
  return __builtin_bit_cast(unsigned, __builtin_convertvector(v, bf16x2));
}
__device__ __forceinline__ float bflo(unsigned u) { return __builtin_bit_cast(float, u << 16); }
__device__ __forceinline__ float bfhi(unsigned u) { return __builtin_bit_cast(float, u & 0xffff0000u); }
__device__ __forceinline__ float rbf(float x) { return bflo(cvtpk(x, x)); }
__device__ __forceinline__ float u2f(unsigned u) { return __builtin_bit_cast(float, u); }
template <int CTRL> __device__ __forceinline__ float dpp(float x) {
  return __builtin_bit_cast(float, __builtin_amdgcn_mov_dpp(__builtin_bit_cast(int, x), CTRL, 0xf, 0xf, true));
}
constexpr int ROR1 = 0x121, ROR2 = 0x122, ROR4 = 0x124, ROR8 = 0x128;
__device__ __forceinline__ float xrow_max(float x) {
  auto s = __builtin_amdgcn_permlane16_swap(__float_as_uint(x), __float_as_uint(x), false, false);
  x = fmaxf(__uint_as_float(s[0]), __uint_as_float(s[1]));
  auto t = __builtin_amdgcn_permlane32_swap(__float_as_uint(x), __float_as_uint(x), false, false);
  return fmaxf(__uint_as_float(t[0]), __uint_as_float(t[1]));
}
__device__ __forceinline__ float xrow_sum(float x) {
  auto s = __builtin_amdgcn_permlane16_swap(__float_as_uint(x), __float_as_uint(x), false, false);
  x = __uint_as_float(s[0]) + __uint_as_float(s[1]);
  auto t = __builtin_amdgcn_permlane32_swap(__float_as_uint(x), __float_as_uint(x), false, false);
  return __uint_as_float(t[0]) + __uint_as_float(t[1]);
}
__device__ __forceinline__ float row_sum(float x) {
  x += dpp<ROR8>(x); x += dpp<ROR4>(x); x += dpp<ROR2>(x); x += dpp<ROR1>(x); return x;
}
__device__ __forceinline__ float wave_sum(float x) { return xrow_sum(row_sum(x)); }
__device__ __forceinline__ v4u ldnt(const void* p) { return __builtin_nontemporal_load((const GAS v4u*)p); }
__device__ __forceinline__ void st_gran(gu64* g, unsigned tag, unsigned v) {
  __hip_atomic_store(g, ((u64)tag << 32) | (u64)v, RLX_AGENT);
}
__device__ __forceinline__ u64 ld_gran(gu64* g) { return __hip_atomic_load(g, RLX_AGENT); }
__device__ __forceinline__ void publish_tile(__amdgpu_buffer_rsrc_t rs, int off, f32x4 a, unsigned tag, int c) {
  const bool hi = c >= 8;
  const v4u v = {__float_as_uint(hi ? a[2] : a[0]), tag, __float_as_uint(hi ? a[3] : a[1]), tag};
  __builtin_amdgcn_raw_buffer_store_b128(v, rs, off + (hi ? 16 : 0), 0, AUX_SC1);
}
__device__ __forceinline__ v4u tile_frag(LAS unsigned char* wa, int ks) {
  const v2u lo = __builtin_bit_cast(v2u, __builtin_amdgcn_ds_read_tr16_b64_v4i16((LAS s16x4*)(wa + ks * 4096)));
  const v2u hi = __builtin_bit_cast(v2u, __builtin_amdgcn_ds_read_tr16_b64_v4i16((LAS s16x4*)(wa + ks * 4096 + 512)));
  return v4u{lo.x, lo.y, hi.x, hi.y};
}
__device__ __forceinline__ void lds_arrive(LAS unsigned* p, int lane) {
  __builtin_amdgcn_fence(__ATOMIC_RELEASE, "workgroup");
  if (lane == 0) __hip_atomic_fetch_add(p, 1u, __ATOMIC_RELAXED, __HIP_MEMORY_SCOPE_WORKGROUP);
}
__device__ __forceinline__ void lds_wait(LAS unsigned* p, unsigned n) {
  while (__hip_atomic_load(p, __ATOMIC_RELAXED, __HIP_MEMORY_SCOPE_WORKGROUP) < n) __builtin_amdgcn_s_sleep(1);
  __builtin_amdgcn_fence(__ATOMIC_ACQUIRE, "workgroup");
}
__device__ __forceinline__ v4u layernorm_row(v4u xv) {
  float xf[8];
#pragma unroll
  for (int k = 0; k < 4; ++k) { xf[2 * k] = bflo(xv[k]); xf[2 * k + 1] = bfhi(xv[k]); }
  float s = 0.f;
#pragma unroll
  for (int k = 0; k < 8; ++k) s += xf[k];
  const float mean = wave_sum(s) * (1.0f / DM);
  float q2 = 0.f;
#pragma unroll
  for (int k = 0; k < 8; ++k) { const float d = xf[k] - mean; q2 = fmaf(d, d, q2); }
  const float var = wave_sum(q2) * (1.0f / DM);
  const float mb = rbf(mean), inv = __builtin_amdgcn_rcpf(rbf(__builtin_amdgcn_sqrtf(rbf(rbf(var) + 1e-5f))));
  v4u o;
#pragma unroll
  for (int k = 0; k < 4; ++k) { const unsigned dd = cvtpk(xf[2 * k] - mb, xf[2 * k + 1] - mb); o[k] = cvtpk(bflo(dd) * inv, bfhi(dd) * inv); }
  return o;
}
}

#define PACE 3835
#define SLOT_GAP 4
#define SLOT_GAP2 1
#define Q_AT 480
#define WO_D0 60
#define WO_STEP 10
#define S2_DELAY 155
#define S4_DELAY 310
#define LATE2 20
#define LATE4 50
#define FINAL_WAVE 5
#define NOW() ((unsigned)__builtin_amdgcn_s_memrealtime())
#define SPIN_PAUSE() do { __builtin_amdgcn_s_sleep(1); asm volatile("" ::: "memory"); } while (0)

__global__ void __launch_bounds__(NTHR, 2)
fused_decode_kernel(const unsigned short* __restrict__ x, const unsigned short* __restrict__ Kc, const unsigned short* __restrict__ Vc,
                    const unsigned short* __restrict__ wq, const unsigned short* __restrict__ wo, const unsigned short* __restrict__ wg,
                    const unsigned short* __restrict__ wu, const unsigned short* __restrict__ wd, unsigned short* __restrict__ out,
                    unsigned char* ws) {
  __shared__ __attribute__((aligned(16))) unsigned char lds_raw[L_END];
  LAS unsigned char* L = (LAS unsigned char*)lds_raw;
  const int tid = threadIdx.x, lane = tid & 63, w = __builtin_amdgcn_readfirstlane(tid >> 6);
  const int g = lane >> 4, c = lane & 15;
  const int j = blockIdx.x, xc = j & 7, idx = j >> 3;
  const int b = idx >> 2, sp = idx & 3;
  const int pair = xc * HKV + (idx >> 2);
  const int h2 = 4 * xc + (idx & 3), cq = idx >> 2;
  const int cb = 4 * xc + (idx & 3);
  const bool isq = (idx & 1) == 0;
  const int fu = xc * 14 + (idx >> 1), fmat = fu & 1;
  const bool has_tile = isq || (idx >> 1) < 14;
  const int tkc = isq ? (idx >> 1) & 1 : (fu >> 1) & 1;
  const int tcol = isq ? 512 * xc + 64 * (idx >> 2) : 64 * (fu >> 2);
  const int tld = isq ? HQ * DH : FF;
  const int q4 = c >> 2, p4 = lane & 3, mt = w & 3;
  const __amdgpu_buffer_rsrc_t rws = __builtin_amdgcn_make_buffer_rsrc((void*)ws, 0, WS_END, RS3);

  const unsigned tstart = NOW();
  unsigned long long pc0; unsigned code_len;
  asm volatile("s_getpc_b64 %0\n.Lfdk_begin:\n\ts_mov_b32 %1, .Lfdk_end-.Lfdk_begin" : "=s"(pc0), "=s"(code_len));
  unsigned epoch = *(const __attribute__((address_space(4))) unsigned*)(ws + WS_EPOCH);
  LAS unsigned* flags = (LAS unsigned*)(L + L_FLAG);
  if (tid < 16) flags[tid] = 0u;
  __syncthreads();

  if (w < 4) {
    const unsigned tag = epoch + 1u;
    const v4u xv0 = *(const GAS v4u*)(x + (2 * w) * DM + lane * 8), xv1 = *(const GAS v4u*)(x + (2 * w + 1) * DM + lane * 8);
    const __amdgpu_buffer_rsrc_t rt = __builtin_amdgcn_make_buffer_rsrc((void*)(isq ? wq : (fmat ? wu : wg)), 0, has_tile ? DM * tld * 2 : 0, RS3);
    const int toff = ((256 * tkc + 64 * w + (lane >> 3)) * tld + tcol + (lane & 7) * 8) * 2;
    v4u ta[8];
#pragma unroll
    for (int u = 0; u < 8; ++u) ta[u] = __builtin_amdgcn_raw_buffer_load_b128(rt, toff, u * 8 * tld * 2, AUX_NT);
    __builtin_amdgcn_sched_barrier(0);
    const v4u o0 = layernorm_row(xv0), o1 = layernorm_row(xv1);
    *(LAS v4u*)(L + L_XN + (2 * w) * XN_STR + lane * 16) = o0;
    *(LAS v4u*)(L + L_XN + (2 * w + 1) * XN_STR + lane * 16) = o1;
#pragma unroll
    for (int u = 0; u < 8; ++u) *(LAS v4u*)(L + L_T0 + w * 8192 + u * 1024 + lane * 16) = ta[u];
    lds_arrive(flags + 1, lane);
    lds_wait(flags + 1, 4u);
    {
      LAS unsigned char* wa = L + L_T0 + (8 * g + q4) * 128 + mt * 32 + 8 * p4;
      LAS unsigned char* xb = L + L_XN + (c & 7) * XN_STR + tkc * 512 + g * 16;
      f32x4 a0 = {0.f, 0.f, 0.f, 0.f}, a1 = {0.f, 0.f, 0.f, 0.f};
#pragma unroll
      for (int ks = 0; ks < 8; ks += 2) {
        a0 = mfma(tile_frag(wa, ks), *(LAS v4u*)(xb + ks * 64), a0);
        a1 = mfma(tile_frag(wa, ks + 1), *(LAS v4u*)(xb + ks * 64 + 64), a1);
      }
      a0 += a1;
      const int dst = isq ? WS_QP + ((tkc * NB + (c & 7)) * 4096) * 8 : WS_GP + (((fmat * 2 + tkc) * NB + (c & 7)) * FF) * 8;
      if (has_tile) publish_tile(rws, dst + (tcol + 16 * mt + 4 * g) * 8, a0, tag, c);
    }
    if (w == 0) {
      int qo = WS_QP + (pair * 512 + 2 * lane) * 8;
      v4u qr[2][4];
      u64 tsg;
      while ((int)(NOW() - tstart) < Q_AT) __builtin_amdgcn_s_sleep(1);
      for (unsigned spins = 0;; ++spins) {
        asm volatile("" : "+v"(qo));
#pragma unroll
        for (int kc = 0; kc < 2; ++kc)
#pragma unroll
          for (int hh = 0; hh < 4; ++hh) qr[kc][hh] = __builtin_amdgcn_raw_buffer_load_b128(rws, qo, (kc * NB * 4096 + hh * 128) * 8, AUX_SC1);
        tsg = ld_gran((gu64*)(ws + WS_TS) + (lane & 7));
        bool ok = true;
#pragma unroll
        for (int kc = 0; kc < 2; ++kc)
#pragma unroll
          for (int hh = 0; hh < 4; ++hh) ok &= (qr[kc][hh][1] == tag) & (qr[kc][hh][3] == tag);
        if (__all(ok) || spins > SPIN_CAP) break;
        SPIN_PAUSE();
      }
#pragma unroll
      for (int hh = 0; hh < 4; ++hh)
        *(LAS unsigned*)(L + L_QS + hh * QS_STR + lane * 4) = cvtpk(u2f(qr[0][hh][0]) + u2f(qr[1][hh][0]), u2f(qr[0][hh][2]) + u2f(qr[1][hh][2]));
      {
        int dt = ((unsigned)(tsg >> 32) == tag) ? (int)((unsigned)tsg - tstart) : 0;
        dt = min(max(dt, -400), 400);
        dt = min(dt, __builtin_amdgcn_mov_dpp(dt, 0xB1, 0xf, 0xf, true)); dt = min(dt, __builtin_amdgcn_mov_dpp(dt, 0x4E, 0xf, 0xf, true)); dt = min(dt, __builtin_amdgcn_mov_dpp(dt, 0x141, 0xf, 0xf, true));
        if (lane == 0) flags[4] = tstart + (unsigned)dt;
      }
      lds_arrive(flags, lane);
    }
  }
  __builtin_amdgcn_sched_barrier(0);

  const size_t kvbase = ((size_t)pair * SK + sp * 256) * (DH * 2);
  const int tn0_ = (w >= 4) ? (w - 4) : (16 + 3 - w), tn1_ = 32 + ((w >= 4) ? (w - 4) : (7 - w));
#define TILE_N(t) ((t) < 4 ? tn0_ + 4 * (t) : tn1_ + 8 * ((t) - 4))
#define TILE_OFF(t) ((TILE_N(t) >> 3) * (1024 * 256) + (TILE_N(t) & 7) * 8192)
  const __amdgpu_buffer_rsrc_t rk = __builtin_amdgcn_make_buffer_rsrc((void*)((const char*)Kc + kvbase), 0, 0x7fffffff, RS3);
  const __amdgpu_buffer_rsrc_t rv = __builtin_amdgcn_make_buffer_rsrc((void*)((const char*)Vc + kvbase), 0, 0x7fffffff, RS3);
  const int koff = (c >> 1) * 256 + (c & 1) * 64 + g * 16;
  const int voff = g * 512 + c * 16;
  v4u buf[4][8];
#define ISSUE(gi) { _Pragma("unroll") for (int u = 0; u < 8; ++u) buf[(gi) & 3][u] = (gi) < 8 \
    ? __builtin_amdgcn_raw_buffer_load_b128(rk, koff + ((u >> 1) & 1) * 2048 + (u & 1) * 128, TILE_OFF((gi) & 7) + (u >> 2) * 4096, AUX_NT) \
    : __builtin_amdgcn_raw_buffer_load_b128(rv, voff + ((u >> 1) & 1) * 2048 + (u & 1) * 256, TILE_OFF((gi) & 7) + (u >> 2) * 4096, AUX_NT); __builtin_amdgcn_sched_barrier(0); }
  unsigned tbase = tstart;
  const unsigned phase = (unsigned)((((xc + (idx >> 2)) & 7) * PACE) >> 10);
#define SLOT_OF(gi) (TILE_N((gi) & 7) + ((gi) >= 8 ? 64 : 0) + ((gi) >= 4 || w < 4 ? SLOT_GAP : 0) + ((gi) >= 6 ? SLOT_GAP2 : 0))
#define PACE_WAIT(gi) { const unsigned due_ = tbase + phase + (unsigned)((SLOT_OF(gi) * PACE) >> 7); while ((int)(NOW() - due_) < 0) __builtin_amdgcn_s_sleep(1); __builtin_amdgcn_sched_barrier(0); }
  PACE_WAIT(0) ISSUE(0)
  asm volatile("" : "+s"(epoch));
  const unsigned tag = epoch + 1u;
  if (w == 4 && idx == 0 && lane == 0) st_gran((gu64*)(ws + WS_TS) + xc, tag, tstart);
  __builtin_amdgcn_sched_barrier(0);
  PACE_WAIT(1) ISSUE(1) PACE_WAIT(2) ISSUE(2) PACE_WAIT(3) ISSUE(3)
  if (w == 6 && lane < 19) {
    const char* pa = lane < 8 ? (const char*)Vc + kvbase + lane * (1024 * 256) : lane < 16 ? (const char*)Kc + kvbase + (lane - 8) * (1024 * 256)
                   : lane == 16 ? (const char*)(wo + (size_t)(128 * h2) * DM + 64 * cq) : lane == 17 ? (const char*)(wd + (size_t)(56 * h2) * DM + 64 * cq) : (const char*)(out + b * DM + 16 * cb);
    const unsigned ptv = __builtin_nontemporal_load((const GAS unsigned*)pa);
    asm volatile("" :: "v"(ptv));
  }
  if (w == 7 && idx < 2) {
    unsigned cv = 0;
#pragma unroll
    for (int k = 0; k < 3; ++k) cv ^= *(const GAS unsigned*)((const char*)pc0 + min((unsigned)(k * 64 + lane) * 128u, code_len - 4u));
    asm volatile("" :: "v"(cv));
  }
  __builtin_amdgcn_sched_barrier(0);
  lds_wait(flags, 1u);
  tbase = __builtin_amdgcn_readfirstlane(flags[4]);

  v4u qf[2];
#pragma unroll
  for (int hf = 0; hf < 2; ++hf) qf[hf] = *(LAS v4u*)(L + L_QS + (c & 3) * QS_STR + hf * 128 + ((c >> 2) & 1) * 64 + g * 16);
  const bool up = (c >> 2) & 1;
  f32x4 acc[8];
#pragma unroll
  for (int t = 0; t < 8; ++t) acc[t] = f32x4{0.f, 0.f, 0.f, 0.f};
  float m = -INFINITY, fw = 0.f;
  v4u sp_[8];

  auto qk = [&](const v4u (&kt)[8], v4u& sc) __attribute__((always_inline)) {
#pragma unroll
    for (int G = 0; G < 4; ++G) {
      f32x4 dg = mfma(kt[2 * G], qf[0], f32x4{0.f, 0.f, 0.f, 0.f});
      dg = mfma(kt[2 * G + 1], qf[1], dg);
      const float e0 = up ? dg[1] : dg[0], e1 = up ? dg[3] : dg[2];
      const float s0 = e0 + dpp<ROR4>(e0), s1 = e1 + dpp<ROR4>(e1);
      const unsigned a0 = cvtpk(s0, s1);
      sc[G] = cvtpk(bflo(a0) * SCALE, bfhi(a0) * SCALE);
      m = fmaxf(m, fmaxf(bflo(sc[G]), bfhi(sc[G])));
    }
    asm volatile("" : "+v"(sc), "+v"(m));
    __builtin_amdgcn_sched_barrier(0);
  };
  auto pv = [&](const v4u (&vt)[8], const v4u& pf) __attribute__((always_inline)) {
#pragma unroll
    for (int t = 0; t < 8; ++t) {
      const unsigned sel = (t & 1) ? 0x07060302u : 0x05040100u;
      v4u a;
#pragma unroll
      for (int mm = 0; mm < 4; ++mm) a[mm] = __builtin_amdgcn_perm(vt[2 * mm + 1][t >> 1], vt[2 * mm][t >> 1], sel);
      acc[t] = mfma(a, pf, acc[t]);
    }
#pragma unroll
    for (int t = 0; t < 8; ++t) asm volatile("" : "+v"(acc[t]));
    __builtin_amdgcn_sched_barrier(0);
  };
#define CONSUME(gi) { if ((gi) < 8) qk(buf[(gi) & 3], sp_[(gi) & 7]); else pv(buf[(gi) & 3], sp_[(gi) & 7]); }

  v4u wt, af[4];
  float gate0 = 0.f, gate1 = 0.f;
  unsigned xfin = 0u;
  const int fo = ((lane >> 3) * NB + b) * DM + 16 * cb + 2 * (lane & 7);
  const int ord = (w >= 4) ? (w - 4) : (7 - w);

  CONSUME(0)
  auto step = [&](auto GI) __attribute__((always_inline)) {
    constexpr int gi = decltype(GI)::value;
    PACE_WAIT(gi) ISSUE(gi)
    if (gi < 15) CONSUME(gi - 3)
    if (gi == 14) CONSUME(12)
    if (gi >= 14) CONSUME(gi - 1)
    if (gi == 10) {
      m = xrow_max(m);
      const float nm = -m * L2E;
      float l0 = 0.f, l1 = 0.f;
#pragma unroll
      for (int t = 0; t < 8; ++t)
#pragma unroll
        for (int G = 0; G < 4; ++G) {
          const float p0 = __builtin_amdgcn_exp2f(fmaf(bflo(sp_[t][G]), L2E, nm)), p1 = __builtin_amdgcn_exp2f(fmaf(bfhi(sp_[t][G]), L2E, nm));
          l0 += p0; l1 += p1;
          sp_[t][G] = cvtpk(p0, p1);
        }
      const float l = xrow_sum(l0 + l1);
      if (lane < 4) *(LAS f32x2*)(L + L_ML + (w * 4 + lane) * 8) = f32x2{m, l};
      lds_arrive(flags + 2, lane);
    }
    if (gi == 5 && w == 1) {
      int go = WS_GP + (b * FF + 56 * h2 + 2 * (lane < 28 ? lane : 27)) * 8;
      v4u gr[2][2];
      for (unsigned spins = 0;; ++spins) {
        asm volatile("" : "+v"(go));
#pragma unroll
        for (int mm = 0; mm < 2; ++mm)
#pragma unroll
          for (int kc = 0; kc < 2; ++kc) gr[mm][kc] = __builtin_amdgcn_raw_buffer_load_b128(rws, go, ((mm * 2 + kc) * NB * FF) * 8, AUX_SC1);
        bool ok = true;
#pragma unroll
        for (int mm = 0; mm < 2; ++mm)
#pragma unroll
          for (int kc = 0; kc < 2; ++kc) ok &= (gr[mm][kc][1] == tag) & (gr[mm][kc][3] == tag);
        if (__all(ok) || spins > SPIN_CAP) break;
        SPIN_PAUSE();
      }
      const float g0 = rbf(u2f(gr[0][0][0]) + u2f(gr[0][1][0])), g1 = rbf(u2f(gr[0][0][2]) + u2f(gr[0][1][2]));
      const float u0 = rbf(u2f(gr[1][0][0]) + u2f(gr[1][1][0])), u1 = rbf(u2f(gr[1][0][2]) + u2f(gr[1][1][2]));
      const float h0 = (g0 > 0.f ? g0 : 0.f * g0) * u0, h1 = (g1 > 0.f ? g1 : 0.f * g1) * u1;
      if (lane < 28) st_gran((gu64*)(ws + WS_HG) + b * 896 + 28 * h2 + lane, tag, cvtpk(h0, h1));
    }
    if (gi == 5)
      wt = ldnt(wd + (size_t)(56 * h2 + 8 * (w < 7 ? w : 6) + (lane >> 3)) * DM + 64 * cq + (lane & 7) * 8);
    if (gi == 7) {
      *(LAS v4u*)(L + L_WD + w * 1024 + lane * 16) = wt;
      lds_arrive(flags + 3, lane);
    }
    if (gi == 11 && w == 0) {
      lds_wait(flags + 2, 8u);
      f32x2 ml[8];
      float M = -INFINITY, Ls = 0.f;
#pragma unroll
      for (int k = 0; k < 8; ++k) { ml[k] = *(LAS f32x2*)(L + L_ML + (k * 4 + (lane & 3)) * 8); M = fmaxf(M, ml[k].x); }
#pragma unroll
      for (int k = 0; k < 8; ++k) Ls = fmaf(__builtin_amdgcn_exp2f((ml[k].x - M) * L2E), ml[k].y, Ls);
      if (lane < 4) st_gran((gu64*)(ws + WS_MG) + (pair * 4 + sp) * 4 + lane, tag, __float_as_uint(fmaf(M, L2E, __builtin_amdgcn_logf(Ls))));
    }
    if (gi == 11 && w >= 4) {
      int ho = WS_HG + ((c & 7) * 896 + 28 * h2 + 4 * g) * 8, ho1 = ho + (g < 3 ? 128 : 0);
      v4u hgr[4];
      for (unsigned spins = 0;; ++spins) {
        asm volatile("" : "+v"(ho), "+v"(ho1));
        hgr[0] = __builtin_amdgcn_raw_buffer_load_b128(rws, ho, 0, AUX_SC1); hgr[1] = __builtin_amdgcn_raw_buffer_load_b128(rws, ho, 16, AUX_SC1);
        hgr[2] = __builtin_amdgcn_raw_buffer_load_b128(rws, ho1, 0, AUX_SC1); hgr[3] = __builtin_amdgcn_raw_buffer_load_b128(rws, ho1, 16, AUX_SC1);
        bool ok = true;
#pragma unroll
        for (int k = 0; k < 4; ++k) ok &= (hgr[k][1] == tag) & (hgr[k][3] == tag);
        if (__all(ok) || spins > SPIN_CAP) break;
        SPIN_PAUSE();
      }
      lds_wait(flags + 3, 8u);
      LAS unsigned char* wa = L + L_WD + (8 * g + q4) * 128 + mt * 32 + 8 * p4;
      const v4u b0 = {hgr[0][0], hgr[0][2], hgr[1][0], hgr[1][2]};
      v4u b1 = {hgr[2][0], hgr[2][2], hgr[3][0], hgr[3][2]};
      if (g == 3) b1 = v4u{0u, 0u, 0u, 0u};
      f32x4 dd = mfma(tile_frag(wa, 0), b0, f32x4{0.f, 0.f, 0.f, 0.f});
      dd = mfma(tile_frag(wa, 1), b1, dd);
      publish_tile(rws, WS_DG + ((h2 * NB + (c & 7)) * DM + 64 * cq + 16 * mt + 4 * g) * 8, dd, tag, c);
    }
    if (gi == 13) {
      gu64* mg = (gu64*)(ws + WS_MG) + pair * 16 + c;
      u64 lg;
      for (unsigned spins = 0;; ++spins) {
        lg = ld_gran(mg);
        if (__all((unsigned)(lg >> 32) == tag) || spins > SPIN_CAP) break;
        SPIN_PAUSE();
      }
      const float ls = u2f((unsigned)lg);
      float mx = fmaxf(ls, dpp<ROR4>(ls)); mx = fmaxf(mx, dpp<ROR8>(mx));
      const float e = __builtin_amdgcn_exp2f(ls - mx), e2 = e + dpp<ROR8>(e), se = e2 + dpp<ROR4>(e2);
      fw = __builtin_amdgcn_exp2f(fmaf(m, L2E, -mx)) * __builtin_amdgcn_rcpf(se);
      asm volatile("" : "+v"(fw));
    }
    if (gi == 14 && w == FINAL_WAVE) {
      int dof = WS_DG + fo * 8;
      v4u dr[4];
      xfin = *(const GAS unsigned*)(x + b * DM + 16 * cb + 2 * (lane & 7));
      for (unsigned spins = 0;; ++spins) {
        asm volatile("" : "+v"(dof));
#pragma unroll
        for (int k = 0; k < 4; ++k) dr[k] = __builtin_amdgcn_raw_buffer_load_b128(rws, dof, k * 8 * NB * DM * 8, AUX_SC1);
        bool ok = true;
#pragma unroll
        for (int k = 0; k < 4; ++k) ok &= (dr[k][1] == tag) & (dr[k][3] == tag);
        if (__all(ok) || spins > SPIN_CAP) break;
        SPIN_PAUSE();
      }
      float d0 = (u2f(dr[0][0]) + u2f(dr[1][0])) + (u2f(dr[2][0]) + u2f(dr[3][0])), d1 = (u2f(dr[0][2]) + u2f(dr[1][2])) + (u2f(dr[2][2]) + u2f(dr[3][2]));
      d0 += dpp<ROR8>(d0); d1 += dpp<ROR8>(d1);
      d0 = rbf(xrow_sum(d0)); d1 = rbf(xrow_sum(d1));
      gate0 = rbf(d0 / (1.0f + __expf(-d0))); gate1 = rbf(d1 / (1.0f + __expf(-d1)));
    }
    __builtin_amdgcn_sched_barrier(0);
  };
  step(IC<4>{}); step(IC<5>{}); step(IC<6>{}); step(IC<7>{}); step(IC<8>{}); step(IC<9>{});
  step(IC<10>{}); step(IC<11>{}); step(IC<12>{}); step(IC<13>{}); step(IC<14>{}); step(IC<15>{});
  const int u = c >> 2;
  LAS unsigned char* rec = L + L_MRG + (c & 3) * 512 + g * 128 + u * 16;
#define PICKA(k) ((u & 1) ? ((u & 2) ? acc[4 + (k)][1] : acc[4 + (k)][0]) : ((u & 2) ? acc[k][1] : acc[k][0]))
#define PICKB(k) ((u & 1) ? ((u & 2) ? acc[4 + (k)][3] : acc[4 + (k)][2]) : ((u & 2) ? acc[k][3] : acc[k][2]))
  f32x4 xA = {0.f, 0.f, 0.f, 0.f}, xB = {0.f, 0.f, 0.f, 0.f};
  if (ord == 7) {
#pragma unroll
    for (int k = 0; k < 7; ++k) {
      lds_wait(flags + 8 + k, 1u);
      xA += *(LAS f32x4*)(rec + k * 2048); xB += *(LAS f32x4*)(rec + k * 2048 + 64);
    }
    asm volatile("" : "+v"(xA), "+v"(xB));
  }
  __builtin_amdgcn_sched_barrier(0);
  CONSUME(15)
  const f32x4 pA = f32x4{PICKA(0), PICKA(1), PICKA(2), PICKA(3)} * fw + xA, pB = f32x4{PICKB(0), PICKB(1), PICKB(2), PICKB(3)} * fw + xB;
  if (ord == 7) {
    const int po = WS_PG + ((pair * 4 + sp) * 256 + (c & 3) * 64) * 8 + g * 128 + u * 16;
    __builtin_amdgcn_raw_buffer_store_b128(v4u{cvtpk(pA[0], pA[1]), tag, cvtpk(pA[2], pA[3]), tag}, rws, po, 0, AUX_SC1);
    __builtin_amdgcn_raw_buffer_store_b128(v4u{cvtpk(pB[0], pB[1]), tag, cvtpk(pB[2], pB[3]), tag}, rws, po + 64, 0, AUX_SC1);
    lds_arrive(flags + 15, lane);
  } else {
    *(LAS f32x4*)(rec + ord * 2048) = pA;
    *(LAS f32x4*)(rec + ord * 2048 + 64) = pB;
    lds_arrive(flags + 8 + ord, lane);
  }
  const unsigned tpub0 = tbase + (unsigned)(((127 + SLOT_GAP + SLOT_GAP2) * PACE) >> 7);
  v4u wl[4];
  {
    if (w >= 4) while ((int)(NOW() - tpub0) < WO_D0 + mt * WO_STEP) __builtin_amdgcn_s_sleep(1);
    const __amdgpu_buffer_rsrc_t rwo = __builtin_amdgcn_make_buffer_rsrc((void*)wo, 0, w >= 4 ? HQ * DH * DM * 2 : 0, RS3);
    const int wof = ((128 * h2 + 32 * mt + (lane >> 3)) * DM + 64 * cq + (lane & 7) * 8) * 2;
#pragma unroll
    for (int k = 0; k < 4; ++k) wl[k] = __builtin_amdgcn_raw_buffer_load_b128(rwo, wof, k * 8 * DM * 2, AUX_NT);
    __builtin_amdgcn_sched_barrier(0);
  }
  {
    const int hf = lane >> 5;
    int po = WS_PG + (((w * HKV + xc) * 4 + hf) * 256 + (h2 & 3) * 64 + 2 * (lane & 31)) * 8;
    v4u gr[2];
    lds_wait(flags + 15, 1u);
    {
      const int t2 = max((int)(S2_DELAY), (int)(NOW() - tpub0) + LATE2);
      while ((int)(NOW() - tpub0) < t2) {}
    }
    gr[0] = __builtin_amdgcn_raw_buffer_load_b128(rws, po, 0, AUX_SC1);
    gr[1] = __builtin_amdgcn_raw_buffer_load_b128(rws, po, 2 * 256 * 8, AUX_SC1);
    if (w >= 4) {
      __builtin_amdgcn_sched_barrier(0);
#pragma unroll
      for (int k = 0; k < 4; ++k) *(LAS v4u*)(L + L_WO + mt * 4096 + k * 1024 + lane * 16) = wl[k];
      lds_arrive(flags + 5, lane);
      __builtin_amdgcn_sched_barrier(0);
    }
    for (unsigned spins = 0;; ++spins) {
      const bool k0 = (gr[0][1] == tag) & (gr[0][3] == tag), k1 = (gr[1][1] == tag) & (gr[1][3] == tag);
      if (__all(k0 & k1) || spins > SPIN_CAP) break;
      asm volatile("" : "+v"(po) :: "memory");
      if (!k0) gr[0] = __builtin_amdgcn_raw_buffer_load_b128(rws, po, 0, AUX_SC1);
      if (!k1) gr[1] = __builtin_amdgcn_raw_buffer_load_b128(rws, po, 2 * 256 * 8, AUX_SC1);
    }
    const float o0 = bflo(gr[0][0]) + bflo(gr[1][0]), o1 = bfhi(gr[0][0]) + bfhi(gr[1][0]), o2 = bflo(gr[0][2]) + bflo(gr[1][2]), o3 = bfhi(gr[0][2]) + bfhi(gr[1][2]);
    auto s01 = __builtin_amdgcn_permlane32_swap(__float_as_uint(o0), __float_as_uint(o2), false, false);
    auto s23 = __builtin_amdgcn_permlane32_swap(__float_as_uint(o1), __float_as_uint(o3), false, false);
    *(LAS unsigned*)(L + L_O + w * QS_STR + (lane & 31) * 8 + hf * 4) = cvtpk(u2f(s01[0]) + u2f(s01[1]), u2f(s23[0]) + u2f(s23[1]));
  }
  if (w < 4) {
    lds_wait(flags + 5, 4u);
    LAS unsigned char* wa = L + L_WO + (8 * g + q4) * 128 + mt * 32 + 8 * p4;
#pragma unroll
    for (int ks = 0; ks < 4; ++ks) { af[ks] = tile_frag(wa, ks); asm volatile("" : "+v"(af[ks])); }
  }
  __syncthreads();
  if (w < 4) {
    LAS unsigned char* ob = L + L_O + (c & 7) * QS_STR + g * 16;
    f32x4 s0 = mfma(af[0], *(LAS v4u*)(ob), f32x4{0.f, 0.f, 0.f, 0.f});
    f32x4 s1 = mfma(af[1], *(LAS v4u*)(ob + 64), f32x4{0.f, 0.f, 0.f, 0.f});
    s0 = mfma(af[2], *(LAS v4u*)(ob + 128), s0);
    s1 = mfma(af[3], *(LAS v4u*)(ob + 192), s1);
    s0 += s1;
    publish_tile(rws, WS_SG + ((h2 * NB + (c & 7)) * DM + 64 * cq + 16 * mt + 4 * g) * 8, s0, tag, c);
  }
  if (w == FINAL_WAVE) {
    int sof = WS_SG + fo * 8;
    v4u sr[4];
    {
      const int t4 = max((int)(S4_DELAY), (int)(NOW() - tpub0) + LATE4);
      while ((int)(NOW() - tpub0) < t4) {}
    }
#pragma unroll
    for (int k = 0; k < 4; ++k) sr[k] = __builtin_amdgcn_raw_buffer_load_b128(rws, sof, k * 8 * NB * DM * 8, AUX_SC1);
    for (unsigned spins = 0;; ++spins) {
      const bool k0 = (sr[0][1] == tag) & (sr[0][3] == tag), k1 = (sr[1][1] == tag) & (sr[1][3] == tag),
                 k2 = (sr[2][1] == tag) & (sr[2][3] == tag), k3 = (sr[3][1] == tag) & (sr[3][3] == tag);
      if (__all(k0 & k1 & k2 & k3) || spins > SPIN_CAP) break;
      asm volatile("" : "+v"(sof) :: "memory");
      if (!k0) sr[0] = __builtin_amdgcn_raw_buffer_load_b128(rws, sof, 0, AUX_SC1);
      if (!k1) sr[1] = __builtin_amdgcn_raw_buffer_load_b128(rws, sof, 8 * NB * DM * 8, AUX_SC1);
      if (!k2) sr[2] = __builtin_amdgcn_raw_buffer_load_b128(rws, sof, 2 * 8 * NB * DM * 8, AUX_SC1);
      if (!k3) sr[3] = __builtin_amdgcn_raw_buffer_load_b128(rws, sof, 3 * 8 * NB * DM * 8, AUX_SC1);
    }
    float s0 = (u2f(sr[0][0]) + u2f(sr[1][0])) + (u2f(sr[2][0]) + u2f(sr[3][0])), s1 = (u2f(sr[0][2]) + u2f(sr[1][2])) + (u2f(sr[2][2]) + u2f(sr[3][2]));
    s0 += dpp<ROR8>(s0); s1 += dpp<ROR8>(s1);
    s0 = rbf(xrow_sum(s0)); s1 = rbf(xrow_sum(s1));
    const unsigned o = cvtpk(bflo(xfin) + rbf(s0 * gate0), bfhi(xfin) + rbf(s1 * gate1));
    if (lane < 8) *(GAS unsigned*)(out + b * DM + 16 * cb + 2 * lane) = o;
    if (j == 0 && lane == 0) __hip_atomic_store((gu32*)(ws + WS_EPOCH), tag, RLX_AGENT);
  }
  asm volatile(".Lfdk_end:");
}

extern "C" void kernel_launch(void* const* d_in, const int* in_sizes, int n_in, void* d_out, int out_size, void* d_ws, size_t ws_size,
                              hipStream_t stream) {
  (void)in_sizes; (void)n_in; (void)out_size; (void)ws_size;
  fused_decode_kernel<<<NWG, NTHR, 0, stream>>>((const unsigned short*)d_in[0], (const unsigned short*)d_in[1], (const unsigned short*)d_in[2],
                                                (const unsigned short*)d_in[3], (const unsigned short*)d_in[4], (const unsigned short*)d_in[6],
                                                (const unsigned short*)d_in[7], (const unsigned short*)d_in[8], (unsigned short*)d_out,
                                                (unsigned char*)d_ws);
}
```
